```python
import torch

def _softmax(x, axis=-1):
    return torch.softmax(x, dim=axis)
def _gelu(x):
    return torch.nn.functional.gelu(x, approximate="tanh")
def _silu(x):
    return torch.nn.functional.silu(x)
def _sigmoid(x):
    return torch.sigmoid(x)
def _layernorm(x, axis=-1, eps=1e-5):
    m = x.mean(dim=axis, keepdim=True)
    v = x.var(dim=axis, keepdim=True, unbiased=False)
    return (x - m) / torch.sqrt(v + eps)
def _scatter_add(tgt, idx, src, axis=0):
    return tgt.clone().index_add_(axis, idx, src)
def _segment_sum(src, seg, n):
    out = torch.zeros((n,) + src.shape[1:], dtype=src.dtype)
    return out.index_add_(0, seg, src)
def _pad(x, pw):
    flat = tuple(v for pair in reversed(pw) for v in pair)
    return torch.nn.functional.pad(x, flat)

kernel_name = 'parallel_sum__ssm_conv_s4d__glu_gated_swiglu__prompt'

def setup_inputs():

    _w = (lambda a: (lambda t: t.to(torch.bfloat16) if t.is_floating_point() else t)(torch.as_tensor(a)))
    def _A_setup():
        Bsz, L, D, N = 1, 2048, 512, 128
        return {
            "u": _w((lambda *s: torch.randn(*s, dtype=torch.bfloat16) * 0.05)(Bsz, L, D)),
            "log_A_real": _w((lambda *s: torch.randn(*s, dtype=torch.bfloat16) * 3.2)(D, N) * 0.5),
            "A_imag": _w((lambda *s: torch.randn(*s, dtype=torch.bfloat16) * 0.05)(D, N) * 3.14),
            "C_real": _w((lambda *s: torch.randn(*s, dtype=torch.bfloat16) * 0.1)(D, N) / N ** 0.5),
            "C_imag": _w((lambda *s: torch.randn(*s, dtype=torch.bfloat16) * 0.2)(D, N) / N ** 0.5),
            "log_dt": _w((lambda *s: torch.randn(*s, dtype=torch.bfloat16) * 0.05)(D) * 0.5 - 4.0),
            "D_skip": _w((lambda *s: torch.randn(*s, dtype=torch.bfloat16) * 0.05)(D)),
        }
    _A_in = _A_setup()

    def _B_setup():
        B, S, D, F = 1, 2048, 512, 1792
        return {
            "x": _w((lambda *s: torch.randn(*s, dtype=torch.bfloat16) * 0.05)(B, S, D)),
            "w1": _w((lambda *s: torch.randn(*s, dtype=torch.bfloat16) * 0.4)(D, F) * (2.0 / D) ** 0.5),
            "w3": _w((lambda *s: torch.randn(*s, dtype=torch.bfloat16) * 0.4)(D, F) * (2.0 / D) ** 0.5),
            "w2": _w((lambda *s: torch.randn(*s, dtype=torch.bfloat16) * 0.4)(F, D) * (2.0 / F) ** 0.5),
        }
    _B_in = _B_setup()

    return {
        "A_u": _A_in["u"],
        "A_log_A_real": _A_in["log_A_real"],
        "A_A_imag": _A_in["A_imag"],
        "A_C_real": _A_in["C_real"],
        "A_C_imag": _A_in["C_imag"],
        "A_log_dt": _A_in["log_dt"],
        "A_D_skip": _A_in["D_skip"],
        "B_x": _B_in["x"],
        "B_w1": _B_in["w1"],
        "B_w3": _B_in["w3"],
        "B_w2": _B_in["w2"],
    }

def reference(A_u, A_log_A_real, A_A_imag, A_C_real, A_C_imag, A_log_dt, A_D_skip, B_x, B_w1, B_w3, B_w2):
    def _A_ref(u, log_A_real, A_imag, C_real, C_imag, log_dt, D_skip):
        L = u.shape[1]
        dt = torch.exp(log_dt)
        A = -torch.exp(log_A_real) + 1j * A_imag
        C = (C_real + 1j * C_imag) * dt[:, None]
        pos = (lambda x, dt: x.to(dt))(torch.arange(L), log_dt.dtype)
        P = torch.exp((A * dt[:, None])[:, :, None] * pos[None, None, :])
        K = 2.0 * torch.einsum("dn,dnl->dl", C, P).real
        L2 = 2 * L
        u_t = (lambda x, *d: x.permute(*d))(u, 0, 2, 1)
        Kf = (lambda x, *a, **k: torch.fft.rfft(x.float(), *a, **k))(K, L2)
        Uf = (lambda x, *a, **k: torch.fft.rfft(x.float(), *a, **k))(u_t, L2)
        y = (lambda x, *a, **k: torch.fft.irfft(x, *a, **k).to(torch.bfloat16))(Uf * Kf[None, :, :], L2)[:, :, :L]
        y = (lambda x, *d: x.permute(*d))(y, 0, 2, 1)
        return y + u * D_skip[None, None, :]

    def _B_ref(x, w1, w3, w2):
        gate = torch.einsum("bsd,df->bsf", x, w1)
        up = torch.einsum("bsd,df->bsf", x, w3)
        h = _silu(gate) * up
        out = torch.einsum("bsf,fd->bsd", h, w2)
        return out

    x = A_u
    xn = _layernorm(x, axis=-1)
    return x + _A_ref(xn, A_log_A_real, A_A_imag, A_C_real, A_C_imag, A_log_dt, A_D_skip) + _B_ref(xn, B_w1, B_w3, B_w2)
```

```cpp
#include <hip/hip_runtime.h>
#include <stdint.h>

typedef unsigned short u16;
typedef unsigned int u32;
typedef unsigned long long u64;
typedef __attribute__((ext_vector_type(4))) short s16x4;
typedef __attribute__((ext_vector_type(2))) u16 u16x2;
typedef __attribute__((ext_vector_type(8))) __bf16 bf16x8;
typedef __attribute__((ext_vector_type(8))) _Float16 f16x8;
typedef __attribute__((ext_vector_type(2))) __bf16 bf16x2;
typedef __attribute__((ext_vector_type(2))) _Float16 f16x2;
typedef __attribute__((ext_vector_type(4))) float f32x4;
typedef __attribute__((ext_vector_type(2))) float f32x2;
typedef __attribute__((ext_vector_type(4))) u32 u32x4;
typedef __attribute__((ext_vector_type(2))) u32 u32x2;
#define LAS __attribute__((address_space(3)))
#define GAS __attribute__((address_space(1)))
#define DEV __device__ __forceinline__
typedef __amdgpu_buffer_rsrc_t rsrc_t;

static constexpr int L = 2048, D = 512, NS = 128, F = 1792;

static constexpr size_t WS_H = 0;
static constexpr size_t WS_XT = 8u << 20;
static constexpr size_t WS_AO = 11u << 20;
static constexpr size_t WS_ST = 14u << 20;
static constexpr size_t WS_CS = WS_ST + (64u << 10);
static constexpr int FS = 128;
static constexpr size_t WS_FX = WS_ST + (128u << 10);
static constexpr size_t WS_FH = WS_FX + 32 * FS;
static constexpr size_t WS_FA = WS_FH + 2048 * FS;
static constexpr size_t WS_EP = WS_FA + 2048 * FS;
static constexpr int H_BYTES = L * F * 2;
static constexpr int XT_BLK = 65536 + 256, XT_BYTES = 32 * XT_BLK;
static constexpr int AO_ROW = 4096 + 128, AO_BYTES = D * AO_ROW;

static constexpr int SLOT = 49152;
static constexpr int LDS_TOTAL = 163840;
static constexpr int SCR = 2 * SLOT;
static constexpr int KG_ROW = 112, KG_PLANE = 128 * KG_ROW;
static constexpr int LDS_KD = SCR + 4 * KG_PLANE;
static constexpr int U_COPY = 4160;
static constexpr int U_CH = 2 * U_COPY;
static constexpr int LDS_U = SCR;
static constexpr int KX_STRIDE = 4608;
static constexpr int LDS_KX = SCR + 2 * U_CH;

struct Params {
  const u16 *x, *lar, *aim, *cre, *cim, *ldt, *dsk, *w1, *w3, *w2;
  u16* out;
  char* ws;
};

DEV float bf2f(u32 v) { return __uint_as_float(v << 16); }
DEV float bfhi(u32 v) { return __uint_as_float(v & 0xffff0000u); }
DEV u32 pk_bf16(float a, float b) { f32x2 p = {a, b}; return __builtin_bit_cast(u32, __builtin_convertvector(p, bf16x2)); }
DEV u32 pk_f16(float a, float b) { f32x2 p = {a, b}; return __builtin_bit_cast(u32, __builtin_convertvector(p, f16x2)); }
DEV float rbf(float a) { return bf2f(pk_bf16(a, a) & 0xffffu); }
DEV float h2f(u32 v) { return (float)__builtin_bit_cast(_Float16, (u16)v); }

template <typename T> DEV T lds_ld(u32 a) { return *(const LAS T*)a; }
template <typename T> DEV void lds_st(u32 a, T v) { *(LAS T*)a = v; }
DEV u32x2 lds_tr(u32 a) { return __builtin_bit_cast(u32x2, __builtin_amdgcn_ds_read_tr16_b64_v4i16((LAS s16x4*)a)); }
DEV u32x4 cat(u32x2 a, u32x2 b) { return __builtin_shufflevector(a, b, 0, 1, 2, 3); }
DEV f32x4 mma_bf(u32x4 a, u32x4 b, f32x4 c) { return __builtin_amdgcn_mfma_f32_16x16x32_bf16(__builtin_bit_cast(bf16x8, a), __builtin_bit_cast(bf16x8, b), c, 0, 0, 0); }
DEV f32x4 mma_h(u32x4 a, u32x4 b, f32x4 c) { return __builtin_amdgcn_mfma_f32_16x16x32_f16(__builtin_bit_cast(f16x8, a), __builtin_bit_cast(f16x8, b), c, 0, 0, 0); }
DEV rsrc_t mk_rsrc(const void* p, u32 bytes) { return __builtin_amdgcn_make_buffer_rsrc((void*)p, 0, bytes, 0x00020000); }
DEV void dma16(rsrc_t rs, u32 voff, u32 sbase, int simm, u32 lbase, int limm) {
  u32 t;
  asm volatile("s_add_u32 m0, %1, %2\n\ts_add_u32 %0, %3, %4\n\tbuffer_load_dwordx4 %5, %6, %0 offen lds"
               : "=&s"(t) : "s"(lbase), "n"(limm), "s"(sbase), "n"(simm), "v"(voff), "s"(rs) : "memory", "scc");
}
#define PINS(x) asm volatile("" : "+s"(x))
#define WAIT_V(n) asm volatile("s_waitcnt vmcnt(%0)" :: "n"(n) : "memory")
#define BAR() asm volatile("s_waitcnt lgkmcnt(0)\n\ts_barrier" ::: "memory")
#define SCHED() __builtin_amdgcn_sched_barrier(0)
#define PIN(x) asm volatile("" : "+v"(x))
#define RLX __ATOMIC_RELAXED, __HIP_MEMORY_SCOPE_AGENT
DEV u64 ld_agent(const char* p) { return __hip_atomic_load((GAS u64*)p, RLX); }
DEV void st_agent(char* p, u64 v) { __hip_atomic_store((GAS u64*)p, v, RLX); }
#define PUBMU 3
static constexpr u32 SPIN_CAP = 1u << 16;

#define STAMP(i) do {} while (0)
#define STAMP_DECL
#define CSTAMP(i)
#define STAMP_OUT
#define STV_ARG
#define STV_PASS

DEV void wait_flags(const char* base, int n, u64 tag, int lane) {
  const char* p = base + (lane < n ? lane : 0) * FS;
#pragma unroll 1
  for (u32 s = 0; s < SPIN_CAP; ++s) {
    u64 v = ld_agent(p);
    if (__all(v == tag)) break;
    __builtin_amdgcn_s_sleep(1);
  }
  asm volatile("" ::: "memory");
}
DEV u64 ld8_async(const char* p) {
  u64 v;
  asm volatile("global_load_dwordx2 %0, %1, off sc1" : "=v"(v) : "v"(p) : "memory");
  return v;
}
DEV u32x4 ld16_async(rsrc_t rs, u32 voff, u32 soff) {
  u32x4 v;
  asm volatile("buffer_load_dwordx4 %0, %1, %2, %3 offen sc1" : "=v"(v) : "v"(voff), "s"(rs), "s"(soff) : "memory");
  return v;
}
DEV u32 ld_u16_async(const u16* base, u32 off) {
  u32 v;
  asm volatile("global_load_ushort %0, %1, %2" : "=v"(v) : "v"(off), "s"(base) : "memory");
  return v;
}
template <int C> DEV float dppf(float v) { return __int_as_float(__builtin_amdgcn_update_dpp(0, __float_as_int(v), C, 0xf, 0xf, true)); }
DEV float row_sum(float v) { v += dppf<0x128>(v); v += dppf<0x124>(v); v += dppf<0x122>(v); v += dppf<0x121>(v); return v; }
DEV float row_max(float v) { v = fmaxf(v, dppf<0x128>(v)); v = fmaxf(v, dppf<0x124>(v)); v = fmaxf(v, dppf<0x122>(v)); v = fmaxf(v, dppf<0x121>(v)); return v; }
DEV float fold32(float a, float b) {
  auto r = __builtin_amdgcn_permlane32_swap(__float_as_uint(a), __float_as_uint(b), false, false);
  return __uint_as_float(r[0]) + __uint_as_float(r[1]);
}
DEV float fold16(float a, float b) {
  auto r = __builtin_amdgcn_permlane16_swap(__float_as_uint(a), __float_as_uint(b), false, false);
  return __uint_as_float(r[0]) + __uint_as_float(r[1]);
}
DEV float dot2bf(u32 a, u32 b, float c) { return __builtin_amdgcn_fdot2_f32_bf16(__builtin_bit_cast(bf16x2, a), __builtin_bit_cast(bf16x2, b), c, false); }
DEV float rdl(float v, int l) { return __int_as_float(__builtin_amdgcn_readlane(__float_as_int(v), l)); }
DEV u64 mk_tag(u64 ep) {
  u32 lo, hi;
  asm volatile("v_readfirstlane_b32 %0, %2\n\tv_readfirstlane_b32 %1, %3" : "=s"(lo), "=s"(hi) : "v"((u32)ep), "v"((u32)(ep >> 32)) : "memory");
  return (((u64)hi << 32) | lo) * 6364136223846793005ull + 1442695040888963407ull;
}

DEV u64 aux_phase(const Params& P, int a, u64 ep, int w, int lane STV_ARG) {
  const int tid = threadIdx.x;
  u32x4 xv[8];
#pragma unroll
  for (int i = 0; i < 8; ++i) xv[i] = *(const u32x4*)(P.x + (size_t)(64 * a + 8 * w + i) * D + lane * 8);
  u32x4 wv[16];
  const int ca = a < 28 ? a : 27;
  const int cmat = ca / 14, cnb = ca % 14;
  rsrc_t rw = mk_rsrc(cmat == 1 ? P.w3 : P.w1, D * F * 2);
  const u32 vo = (tid >> 4) * (F * 2) + cnb * 256 + (tid & 15) * 16;
  WAIT_V(8);
  const u64 tag = mk_tag(ep);
  STAMP(16);
  float s1[8], s2[8];
#pragma unroll
  for (int i = 0; i < 8; ++i) {
    s1[i] = 0.f; s2[i] = 0.f;
#pragma unroll
    for (int j = 0; j < 4; ++j) { s1[i] = dot2bf(xv[i][j], 0x3f803f80u, s1[i]); s2[i] = dot2bf(xv[i][j], xv[i][j], s2[i]); }
    wv[i] = __builtin_amdgcn_raw_buffer_load_b128(rw, vo, i * 32 * F * 2, 0);
    SCHED();
  }
  float rs[2], mr[2];
#pragma unroll
  for (int g = 0; g < 2; ++g) {
    float S = row_sum(fold16(fold32(s1[4 * g], s1[4 * g + 2]), fold32(s1[4 * g + 1], s1[4 * g + 3])));
    float Q = row_sum(fold16(fold32(s2[4 * g], s2[4 * g + 2]), fold32(s2[4 * g + 1], s2[4 * g + 3])));
    float mf = S * (1.f / D);
    float var = rbf(fmaxf(Q * (1.f / D) - mf * mf, 0.f));
    float m = rbf(mf);
    float sd = rbf(__builtin_amdgcn_sqrtf(rbf(var + 1e-5f)));
    rs[g] = __builtin_amdgcn_rcpf(sd);
    mr[g] = -m * rs[g];
    if ((lane & 15) == 0)
      st_agent(P.ws + WS_ST + (size_t)(64 * a + 8 * w + 4 * g + (lane >> 4)) * 8,
               ((u64)(u32)tag << 32) | (__float_as_uint(sd) & 0xffff0000u) | (__float_as_uint(m) >> 16));
  }
  STAMP(17);
  float rr[8], cc[8];
#pragma unroll
  for (int i = 0; i < 8; ++i) { rr[i] = rdl(rs[i >> 2], (i & 3) * 16); cc[i] = rdl(mr[i >> 2], (i & 3) * 16); }
#pragma unroll
  for (int e = 0; e < 8; ++e) {
    u32x4 o;
#pragma unroll
    for (int j = 0; j < 4; ++j) {
      const u32 v0 = xv[2 * j][e >> 1], v1 = xv[2 * j + 1][e >> 1];
      o[j] = pk_f16(fmaf((e & 1) ? bfhi(v0) : bf2f(v0), rr[2 * j], cc[2 * j]), fmaf((e & 1) ? bfhi(v1) : bf2f(v1), rr[2 * j + 1], cc[2 * j + 1]));
    }
    lds_st<u32x4>((u32)((8 * lane + e) * 128 + ((w ^ (lane & 7)) * 16)), o);
    wv[8 + e] = __builtin_amdgcn_raw_buffer_load_b128(rw, vo, (8 + e) * 32 * F * 2, 0);
    SCHED();
  }
  STAMP(18);
  BAR();
  STAMP(19);
  rsrc_t rx = mk_rsrc(P.ws + WS_XT, XT_BYTES);
#pragma unroll
  for (int j = 0; j < 8; ++j) {
    int d = (tid >> 3) + 64 * j, s = tid & 7;
    u32x4 o = lds_ld<u32x4>((u32)(d * 128 + ((s ^ ((d >> 3) & 7)) * 16)));
    __builtin_amdgcn_raw_buffer_store_b128(o, rx, a * XT_BLK + d * 128 + s * 16, 0, 16);
  }
  STAMP(20);
  {
    u32x4 sel;
    const int i = lane & 15;
    const u32 one = (i & 1) ? 0x3f800000u : 0x3f80u;
    sel[0] = (i >> 1) == 0 ? one : 0; sel[1] = (i >> 1) == 1 ? one : 0;
    sel[2] = (i >> 1) == 2 ? one : 0; sel[3] = (i >> 1) == 3 ? one : 0;
    f32x4 c0 = {0.f, 0.f, 0.f, 0.f}, c1 = c0;
#pragma unroll
    for (int k = 0; k < 16; k += 2) { c0 = mma_bf(sel, wv[k], c0); c1 = mma_bf(sel, wv[k + 1], c1); }
    if (lane < 32) lds_st<f32x4>(65536 + w * 512 + (lane & 15) * 32 + (lane >> 4) * 16, c0 + c1);
  }
  STAMP(21);
  WAIT_V(0);
  STAMP(22);
  BAR();
  if (tid == 0) st_agent(P.ws + WS_FX + a * FS, tag);
  if (tid < 128) {
    float s = 0.f;
#pragma unroll
    for (int j = 0; j < 8; ++j) s += lds_ld<float>(65536 + j * 512 + tid * 4);
    st_agent(P.ws + WS_CS + (size_t)(cmat * F + cnb * 128 + tid) * 8, ((u64)(u32)tag << 32) | __float_as_uint(s));
  }
  BAR();
  return tag;
}

struct cpx { float r, i; };
struct cpx2 { f32x2 r, i; };
DEV cpx cmul(cpx a, cpx b) { return cpx{fmaf(a.r, b.r, -(a.i * b.i)), fmaf(a.r, b.i, a.i * b.r)}; }
DEV cpx csqr(cpx a) { return cpx{fmaf(a.r, a.r, -(a.i * a.i)), (a.r + a.r) * a.i}; }
DEV cpx2 cmul2(cpx2 a, cpx b) {
  f32x2 br = {b.r, b.r}, bi = {b.i, b.i};
  return cpx2{__builtin_elementwise_fma(a.r, br, -(a.i * bi)), __builtin_elementwise_fma(a.r, bi, a.i * br)};
}
DEV cpx2 cpair(cpx a, cpx b) { return cpx2{f32x2{a.r, b.r}, f32x2{a.i, b.i}}; }
DEV cpx hi_of(cpx2 a) { return cpx{a.r[1], a.i[1]}; }
DEV cpx cexp_at(float ar2, float airev, float p) {
  float e = __builtin_amdgcn_exp2f(ar2 * p);
  float fr = __builtin_amdgcn_fractf(airev * p);
  return cpx{e * __builtin_amdgcn_cosf(fr), e * __builtin_amdgcn_sinf(fr)};
}
struct KgIn { u32 lar, aim, cr, ci, cr2, ci2, ldt, dsk; };
struct KgVal { u32 re[16], im[16]; float inv, dsk; };
DEV KgIn kgen_load(const Params& P, int c, int w, int lane) {
  const int d = 2 * c + (w >> 2);
  const u32 o = (d * NS + 64 * ((w >> 1) & 1) + lane) * 2, o2 = o ^ 128, od = d * 2;
  KgIn k;
  k.lar = ld_u16_async(P.lar, o); k.aim = ld_u16_async(P.aim, o);
  k.cr = ld_u16_async(P.cre, o); k.ci = ld_u16_async(P.cim, o);
  k.cr2 = ld_u16_async(P.cre, o2); k.ci2 = ld_u16_async(P.cim, o2);
  k.ldt = ld_u16_async(P.ldt, od); k.dsk = ld_u16_async(P.dsk, od);
  return k;
}
template <typename FN> DEV KgVal kgen_values(KgIn k, int w, FN&& side) {
  asm volatile("" : "+v"(k.lar), "+v"(k.aim), "+v"(k.cr), "+v"(k.ci), "+v"(k.cr2), "+v"(k.ci2), "+v"(k.ldt), "+v"(k.dsk));
  KgVal o;
  float cr = bf2f(k.cr), ci = bf2f(k.ci), cr2 = bf2f(k.cr2);
  float dt = rbf(__expf(bf2f(k.ldt)));
  float ar = -rbf(__expf(bf2f(k.lar))) * dt, ai = bf2f(k.aim) * dt;
  float ar2 = ar * 1.44269504088896f, airev = ai * 0.159154943091895f;
  side(0);
  float mx = row_max(fmaxf(fabsf(cr) + fabsf(ci), fabsf(cr2) + fabsf(bf2f(k.ci2))));
  float sm = row_sum(cr + cr2);
  o.dsk = bf2f(k.dsk) + 2.f * dt * ((rdl(sm, 0) + rdl(sm, 16)) + (rdl(sm, 32) + rdl(sm, 48)));
  side(1);
  u32 mb = max(max((u32)__builtin_amdgcn_readlane(__float_as_int(mx), 0), (u32)__builtin_amdgcn_readlane(__float_as_int(mx), 16)),
               max((u32)__builtin_amdgcn_readlane(__float_as_int(mx), 32), (u32)__builtin_amdgcn_readlane(__float_as_int(mx), 48)));
  float M = 2.f * dt * __uint_as_float(mb);
  int sexp = 261 - (int)((__float_as_uint(M) >> 23) & 0xff);
  sexp = sexp > 254 ? 254 : sexp;
  o.inv = __uint_as_float((u32)(254 - sexp) << 23);
  float cs = 2.f * dt * __uint_as_float((u32)sexp << 23);
  cpx C{cr * cs, ci * cs};
  side(2);
  cpx w128 = cexp_at(ar2, airev, 128.f), w256 = csqr(w128);
  side(3);
  cpx2 V[16];
  if (w & 1) {
    cpx w1 = cexp_at(ar2, airev, 1.f), w2 = csqr(w1), w4 = csqr(w2), w512 = csqr(w256);
    V[0] = cmul2(cpair(w1, w2), C);
    V[1] = cmul2(V[0], w2); V[2] = cmul2(V[0], w4); V[3] = cmul2(V[1], w4);
#pragma unroll
    for (int r = 0; r < 4; ++r) V[4 + r] = cmul2(V[r], w128);
    V[8] = cmul2(cpair(hi_of(V[0]), hi_of(V[1])), w256);
    V[9] = cmul2(V[8], w4);
    V[10] = cmul2(V[8], w128); V[11] = cmul2(V[9], w128);
    V[12] = cmul2(cpair(hi_of(V[1]), hi_of(V[3])), w512);
    V[13] = cmul2(V[12], w128); V[14] = cmul2(V[12], w256); V[15] = cmul2(V[13], w256);
  } else {
    cpx g = cmul(C, cexp_at(ar2, airev, 1032.f));
    V[0] = cpair(g, cmul(g, w128));
#pragma unroll
    for (int r = 1; r < 4; ++r) V[r] = cmul2(V[r - 1], w256);
    cpx y8 = cexp_at(ar2, -airev, 8.f), y16 = csqr(y8), y32 = csqr(y16), y64 = csqr(y32);
    V[4] = cpair(cpx{1.f, 0.f}, y8);
    V[5] = cmul2(V[4], y16); V[6] = cmul2(V[4], y32); V[7] = cmul2(V[5], y32);
#pragma unroll
    for (int r = 0; r < 4; ++r) V[8 + r] = cmul2(V[4 + r], y64);
#pragma unroll
    for (int r = 12; r < 16; ++r) V[r] = V[r - 12];
  }
#pragma unroll
  for (int r = 0; r < 16; ++r) { if ((r & 7) == 0) side(4 + (r >> 3)); o.re[r] = pk_f16(V[r].r[0], V[r].r[1]); o.im[r] = pk_f16(V[r].i[0], V[r].i[1]); }
  return o;
}
DEV void kgen_store(const KgVal& v, int c, u32 base) {
  lds_st<u32x4>(base, u32x4{v.re[4 * c], v.re[4 * c + 1], v.re[4 * c + 2], v.re[4 * c + 3]});
  lds_st<u32x4>(base + KG_PLANE, u32x4{v.im[4 * c], v.im[4 * c + 1], v.im[4 * c + 2], v.im[4 * c + 3]});
}
struct KdTab { u16 v[2048]; };
constexpr KdTab make_kd() {
  KdTab t{};
  for (int i = 0; i < 2048; ++i) {
    int l = (i & ~7) + 7 - (i & 7);
    if (l == 0) l = 1;
    int sh = l < 256 ? 0 : l < 512 ? 1 : l < 1024 ? 2 : 3;
    int q = l >> sh, rem = l - (q << sh), half = (1 << sh) >> 1;
    if (sh > 0 && (rem > half || (rem == half && (q & 1)))) ++q;
    int p = (q << sh) - 1;
    int I = p >> 7, sg = I < 2 ? 0 : I < 4 ? 1 : I < 8 ? 2 : 3;
    int r = 8 * sg + (I << (3 - sg)) + ((p & 7) >> sg);
    t.v[i] = (u16)((r * 16 + ((p >> 3) & 15)) * 2);
  }
  return t;
}
__constant__ __attribute__((aligned(16))) KdTab kd_tab = make_kd();

DEV void w2_piece(rsrc_t rw2, u32 vw2, u32 sw2, int k0, int i, int slot, u32 lw2) {
  dma16(rw2, vw2, sw2, (k0 * 2 + i) * 16384, lw2, slot + i * 2048);
}

#define HW 10
constexpr int g2_ops_at(int s) { return (HW + 2 * s < 28 ? 2 : 0) + (s == 1 || s == 2 ? 1 : 0) + (s >= 3 && s <= 6 ? 1 : 0) + (s == 6 ? 1 : 0) + (s == 10 ? 1 : 0) + (s == 12 ? 8 : 0); }
constexpr int g2_issued_through(int s) { int n = HW; for (int i = 0; i <= s; ++i) n += g2_ops_at(i); return n; }
constexpr int g2_pos_hf(int i) { return i < HW ? i + 1 : g2_issued_through((i - HW) / 2 - 1) + 1 + ((i - HW) & 1); }
constexpr int g2_wait_hf(int i, int s) { return g2_issued_through(s - 1) + (HW + 2 * s < 28 ? 2 : 0) - g2_pos_hf(i); }
constexpr u64 g2_pack() { u64 v = 0; for (int s = 0; s < 12; ++s) v |= (u64)g2_wait_hf(2 * s + 1, s) << (5 * s); return v; }
static constexpr u64 G2W = g2_pack();
static constexpr int G2_AO = g2_issued_through(6) - 1 - g2_issued_through(2);

__global__ __launch_bounds__(512, 2) void fused(Params P) {
  const int b = blockIdx.x, xcd = b & 7, idx = b >> 3;
  const int tid = threadIdx.x, lane = tid & 63;
  const int w = __builtin_amdgcn_readfirstlane(tid >> 6);
  STAMP_DECL;
  STAMP(0); CSTAMP(58);
  KgIn kin = kgen_load(P, b, w, lane);
  u64 ep;
  asm volatile("global_load_dwordx2 %0, %1, %2 sc1" : "=v"(ep) : "v"((u32)WS_EP), "s"(P.ws) : "memory");
  STAMP(12);
  const bool is_aux = idx >= 28;
  const int mb = (xcd & 3) * 4 + (idx & 3), nb = (xcd >> 2) * 7 + (idx >> 2);
  const int tt = xcd * 4 + (idx & 3), nb2 = idx >> 2;
  const int th = w & 1, kq = w >> 1;
  const int ch = w >> 2, v4 = w & 3;
  const int lc = lane & 15, lg = lane >> 4, lq = (lane & 15) >> 2, lp = lane & 3;
  rsrc_t rx = mk_rsrc(P.x, L * D * 2);
  rsrc_t rxg = mk_rsrc(P.x, is_aux ? 0 : L * D * 2);
  rsrc_t rw = mk_rsrc(w >= 4 ? P.w3 : P.w1, is_aux ? 0 : D * F * 2);
  const u32 vx0 = (lane >> 3) * 1024 + (((lane & 7) ^ lg) * 16);
  const u32 vx1 = vx0 ^ 64;
  const u32 vw0 = lg * (F * 2) + ((lc ^ (lg << 1)) * 16);
  const u32 vw1 = vw0 ^ 128;
  const u32 sx0 = (u32)((mb * 128 + 16 * w) * 1024), sw0 = (u32)(16 * (w & 3) * (F * 2) + nb * 256);
  const u32 lx0 = w * 2048, lw0 = w * 4096;
  auto g1_piece = [&](int s, int i) {
    const int slot = ((s + 1) % 3) * SLOT;
    if (i == 0) dma16(rxg, vx0, sx0, s * 128, lx0, slot);
    else if (i == 1) dma16(rxg, vx1, sx0, s * 128 + 8192, lx0, slot + 1024);
    else dma16(rw, i < 4 ? vw0 : vw1, sw0, (s * 64 + (i - 2) * 4) * F * 2, lw0, slot + 16384 + (i - 2) * 1024);
  };

  u64 tag;
  if (__builtin_expect(!is_aux, 1)) {
#pragma unroll
    for (int i = 0; i < 6; ++i) g1_piece(0, i);
    STAMP(13);
    WAIT_V(6);
    tag = mk_tag(ep);
  } else {
    tag = aux_phase(P, xcd * 4 + idx - 28, ep, w, lane STV_PASS);
  }
  STAMP(1);
  KgVal kv = kgen_values(kin, w, [&](int i) { g1_piece(1, i); });
  STAMP(2); CSTAMP(59);
  rsrc_t rw2 = mk_rsrc(P.w2, F * D * 2);
  const u32 vw2 = (lane >> 3) * 1024 + (((lane & 7) ^ (lg << 1)) * 16);
  u32 sw2 = (u32)((kq * 448 + th * 8) * 1024 + nb2 * 128), lw2 = kq * 12288 + th * 1024;
  PINS(sw2); PINS(lw2);
  u32x4 uv, kdv; u32 unx;
  auto load_u = [&]() {
    rsrc_t rxt = mk_rsrc(P.ws + WS_XT, XT_BYTES);
    const int g = tid & 255, gn = g + 1;
    const u32 cb = (2 * b + ch) * 128;
    uv = ld16_async(rxt, (g >> 3) * XT_BLK + (g & 7) * 16 + cb, 0);
    asm volatile("buffer_load_dword %0, %1, %2, 0 offen sc1" : "=v"(unx) : "v"((gn >> 3) * XT_BLK + (gn & 7) * 16 + cb), "s"(rxt) : "memory");
    asm volatile("global_load_dwordx4 %0, %1, %2" : "=v"(kdv) : "v"(g * 16), "s"(&kd_tab) : "memory");
  };
  auto kt_store = [&](int c) {
    const u32 kbase = SCR + ch * 2 * KG_PLANE + (64 * ((w >> 1) & 1) + lane) * KG_ROW;
    kgen_store(kv, c, kbase + (c < 3 ? ((w & 1) ^ 1) * 64 + c * 16 : 64 - (w & 1) * 16));
  };

  f32x4 acc[4][4];
#pragma unroll
  for (int i = 0; i < 16; ++i) acc[i >> 2][i & 3] = f32x4{0.f, 0.f, 0.f, 0.f};
  u32 sdv[4] = {0x3f800000u, 0x3f800000u, 0x3f800000u, 0x3f800000u};

  if (__builtin_expect(!is_aux, 1)) {
    u32 xa[2], wa[2];
    {
      const int row = 64 * th + lc;
      xa[0] = SLOT + row * 128 + ((lg ^ ((row >> 1) & 7)) * 16);
      xa[1] = xa[0] ^ 64;
      wa[0] = SLOT + 16384 + (8 * lg + lq) * 256 + (((2 * kq) ^ (lq | ((lg & 1) << 2))) * 32) + lp * 8;
      wa[1] = wa[0] ^ 32;
    }
    const char* pcs = P.ws + WS_CS + (size_t)((lg >> 1) * F + nb * 128 + kq * 32 + (lg & 1) * 16 + lc) * 8;
    const char* pst = P.ws + WS_ST + (size_t)(mb * 128 + 64 * th + lc) * 8;
    u64 gc, gs[4], fx;
    const char* pfx = P.ws + WS_FX + (4 * w + lp) * FS;
    WAIT_V(6);
    BAR();
    STAMP(3);
    u32x4 cx[4], cw[4], nx[4];
    u32x2 nw[8];
    auto rd = [&](int i, int ks) {
      if (i < 4) nx[i] = lds_ld<u32x4>(xa[ks] + i * 2048);
      else nw[i - 4] = lds_tr(wa[((i - 4) >> 1) & 1] + ((i - 4) >> 2) * 16384 + ks * 8192 + ((i - 4) & 1) * 1024);
    };
    auto rot = [&]() {
#pragma unroll
      for (int i = 0; i < 4; ++i) { cx[i] = nx[i]; cw[i] = cat(nw[2 * i], nw[2 * i + 1]); }
    };
#pragma unroll
    for (int i = 0; i < 12; ++i) rd(i, 0);
#pragma unroll
    for (int i = 0; i < 3; ++i) g1_piece(2, i);
    rot();
#pragma unroll
    for (int s = 0; s < 8; ++s) {
#pragma unroll
      for (int ks = 0; ks < 2; ++ks) {
        if (ks == 1) {
          if (s >= 5) STAMP(16 + 2 * s);
          WAIT_V(6);
          if (s == 7) {
            const u32 t32 = (u32)tag;
            PIN(fx); PIN(gc); PIN(gs[0]); PIN(gs[1]); PIN(gs[2]); PIN(gs[3]);
            auto ok = [&]() {
              return __all((((u32)(gc >> 32) ^ t32) | ((u32)(gs[0] >> 32) ^ t32) | ((u32)(gs[1] >> 32) ^ t32) | ((u32)(gs[2] >> 32) ^ t32) |
                            ((u32)(gs[3] >> 32) ^ t32) | ((u32)fx ^ t32) | ((u32)(fx >> 32) ^ (u32)(tag >> 32))) == 0);
            };
            if (__builtin_expect(!ok(), 0)) {
#pragma unroll 1
              for (u32 sp = 0; sp < SPIN_CAP; ++sp) {
                gc = ld_agent(pcs); fx = ld_agent(pfx);
#pragma unroll
                for (int t = 0; t < 4; ++t) gs[t] = ld_agent(pst + t * 128);
                if (ok()) break;
                __builtin_amdgcn_s_sleep(1);
              }
            }
          }
          BAR();
          if (s < 7) {
            const int dl = (s + 2) % 3 == 0 ? -2 * SLOT : SLOT;
            xa[0] += dl; xa[1] += dl; wa[0] += dl; wa[1] += dl;
            PIN(xa[0]); PIN(xa[1]); PIN(wa[0]); PIN(wa[1]);
          }
          if (s >= 5) STAMP(17 + 2 * s);
        }
        const int rs = ks == 1 ? s : s - 1;
#pragma unroll
        for (int i = 0; i < 16; ++i) {
          SCHED();
          acc[i >> 2][i & 3] = mma_bf(cw[i >> 2], cx[i & 3], acc[i >> 2][i & 3]);
          SCHED();
          static constexpr int order[12] = {0, 4, 5, 1, 2, 3, 6, 7, 8, 9, 10, 11};
          if (i < 12 && !(s == 7 && ks == 1)) rd(order[i], ks ^ 1);
          if ((i == 2 || i == 7 || i == 12)) {
            const int pi = (ks == 1 ? 0 : 3) + i / 5;
            if (rs + 3 < 8) g1_piece(rs + 3, pi);
            else if (rs == 5) w2_piece(rw2, vw2, sw2, 0, pi, 0, lw2);
            else if (rs == 6) w2_piece(rw2, vw2, sw2, 3, pi, SLOT, lw2);
          }
          if (s == 6 && ks == 1 && i == 0) {
            gc = ld8_async(pcs);
#pragma unroll
            for (int t = 0; t < 4; ++t) gs[t] = ld8_async(pst + t * 128);
            fx = ld8_async(pfx);
          }
          if (s == 7 && ks == 1) {
            if (i == 0) load_u();
            if ((i & 3) == 2) kt_store(i >> 2);
          }
        }
        SCHED();
        rot();
      }
    }
    STAMP(4);
    const float cv = __uint_as_float((u32)gc);
    const float c0 = rbf(cv), r1 = cv - c0, c1 = rbf(r1);
    const u32 e0 = pk_bf16(c0, c1), e1 = pk_bf16(r1 - c1, 0.f);
#pragma unroll
    for (int t = 0; t < 4; ++t) {
      const u32 m = ((u32)gs[t] & 0xffffu) ^ 0x8000u;
      const u32x4 mv = {m | (m << 16), m, 0, 0};
#pragma unroll
      for (int a = 0; a < 4; ++a) acc[a][t] = mma_bf(u32x4{lg == a ? e0 : 0u, lg == a ? e1 : 0u, 0, 0}, mv, acc[a][t]);
      sdv[t] = (u32)gs[t] & 0xffff0000u;
    }
  } else {
    WAIT_V(0);
    BAR();
#pragma unroll
    for (int i = 0; i < 6; ++i) w2_piece(rw2, vw2, sw2, 0, i, 0, lw2);
#pragma unroll
    for (int i = 0; i < 6; ++i) w2_piece(rw2, vw2, sw2, 3, i, SLOT, lw2);
#pragma unroll
    for (int i = 0; i < 4; ++i) kt_store(i);
    wait_flags(P.ws + WS_FX + 4 * w * FS, 4, tag, lane);
    BAR();
    load_u();
    WAIT_V(0);
  }
  STAMP(5); CSTAMP(60);
  BAR();
  STAMP(32);
  u32 w2a[4];
#pragma unroll
  for (int g = 0; g < 4; ++g) w2a[g] = kq * 12288 + (4 * lg + lq) * 128 + (((g ^ kq) ^ ((2 * lg + (lq >> 1)) & 3)) * 32) + lp * 8;
  u32x4 wr[6][4];
  auto w2_frag = [&](u32 off, int g) { return cat(lds_tr(w2a[g] + off), lds_tr(w2a[g] + off + 2048)); };
  u32x2 wh[6][8];
  auto w2_rd = [&](int i) {
    const int t = i >> 3, g = (i >> 1) & 3;
    wh[t][i & 7] = lds_tr(w2a[g] + (t / 3) * SLOT + (t % 3) * 4096 + (i & 1) * 2048);
    if (i & 1) wr[t][g] = cat(wh[t][(i & 7) - 1], wh[t][i & 7]);
  };
  {
    rsrc_t rhs = mk_rsrc(P.ws + WS_H, is_aux ? 0 : H_BYTES);
    const u32 ho = (u32)(((mb * 8 + th * 4) * 56 + nb * 4 + kq) * 1024 + lane * 16);
    auto epi = [&](int t, auto&& side) {
      const float sd = __uint_as_float(sdv[t]);
      float ke = -1.44269504088896f * __builtin_amdgcn_rcpf(sd), s2 = sd * sd;
      asm("" : "+v"(s2), "+v"(ke));
      f32x2 e[4], gu[4];
#pragma unroll
      for (int j = 0; j < 4; ++j) {
        const f32x2 g = {acc[j >> 1][t][2 * (j & 1)], acc[j >> 1][t][2 * (j & 1) + 1]};
        const f32x2 u = {acc[2 + (j >> 1)][t][2 * (j & 1)], acc[2 + (j >> 1)][t][2 * (j & 1) + 1]};
        e[j] = g * ke;
        gu[j] = g * u;
        SCHED(); side(j); SCHED();
      }
#pragma unroll
      for (int j = 0; j < 8; ++j) { e[j >> 1][j & 1] = __builtin_amdgcn_exp2f(e[j >> 1][j & 1]); SCHED(); side(4 + j); SCHED(); }
#pragma unroll
      for (int j = 0; j < 4; ++j) { e[j] = __builtin_elementwise_fma(e[j], f32x2{s2, s2}, f32x2{s2, s2}); SCHED(); side(12 + j); SCHED(); }
#pragma unroll
      for (int j = 0; j < 8; ++j) { e[j >> 1][j & 1] = __builtin_amdgcn_rcpf(e[j >> 1][j & 1]); SCHED(); side(16 + j); SCHED(); }
      u32x4 o;
#pragma unroll
      for (int j = 0; j < 4; ++j) { gu[j] *= e[j]; SCHED(); side(24 + j); SCHED(); }
#pragma unroll
      for (int j = 0; j < 4; ++j) o[j] = pk_bf16(gu[j][0], gu[j][1]);
      __builtin_amdgcn_raw_buffer_store_b128(o, rhs, ho + t * 56 * 1024, 0, 16);
      SCHED();
    };
    const int kt = v4 < 3 ? v4 : 0;
    const u32 krb = SCR + ch * 2 * KG_PLANE + (8 * lg + lq) * KG_ROW + lp * 8, kra = krb + kt * 32;
    u32x2 kf[8][4];
    f32x4 ka = {0.f, 0.f, 0.f, 0.f}, kb = ka;
    auto kt_rd = [&](int i) { kf[i >> 2][i & 3] = lds_tr(((i & 2) ? krb + 80 : kra) + (i >> 2) * 32 * KG_ROW + (i & 1) * 4 * KG_ROW); };
    auto kt_mma = [&](int k) {
      if (k & 1) kb = mma_h(cat(kf[k][2], kf[k][3]), cat(kf[k][0], kf[k][1]), kb);
      else ka = mma_h(cat(kf[k][2], kf[k][3]), cat(kf[k][0], kf[k][1]), ka);
    };
#pragma unroll
    for (int i = 0; i < 4; ++i) kt_rd(i);
    SCHED();
    epi(0, [&](int k) { kt_rd(4 + k); if (k >= 8 && (k & 3) == 1) kt_mma((k - 8) >> 2); });
    epi(1, [&](int k) {
      if (k == 5 || k == 7 || k == 9) kt_mma(5 + ((k - 5) >> 1));
      if (k == 20) {
        ka += kb;
        lds_st<u32x2>(LDS_KD + ch * 1536 + ((16 * kt + lc) * 16 + 4 * lg) * 2, u32x2{pk_f16(ka[0], ka[1]), pk_f16(ka[2], ka[3])});
      }
      if (k < 20) w2_rd(k);
    });
    STAMP(34);
    WAIT_V(2);
    BAR();
    STAMP(35);
    PIN(uv); PIN(unx); PIN(kdv);
    const int g = tid & 255;
    u32 kg[8];
    epi(2, [&](int k) {
      if (k < 8) kg[k] = lds_ld<u16>(LDS_KD + ch * 1536 + ((k & 1) ? kdv[k >> 1] >> 16 : kdv[k >> 1] & 0xffffu));
      if (k == 9) lds_st<u32x4>(LDS_U + ch * U_CH + 32 + g * 16, uv);
      if (k == 14) lds_st<u32x4>(LDS_U + ch * U_CH + U_COPY + 32 + g * 16,
                                 u32x4{__builtin_amdgcn_alignbit(uv[1], uv[0], 16), __builtin_amdgcn_alignbit(uv[2], uv[1], 16),
                                       __builtin_amdgcn_alignbit(uv[3], uv[2], 16), __builtin_amdgcn_alignbit(unx, uv[3], 16)});
      if (k == 19) {
        const u32 za = g < 4 ? LDS_U + ch * U_CH + ((g + 3) & 1) * 16 + (((g + 3) >> 1) & 1) * U_COPY : LDS_KX + ch * KX_STRIDE + (g & 31) * 16;
        lds_st<u32x4>(za, u32x4{0, 0, 0, g == 0 ? uv[0] << 16 : 0u});
      }
      if (k == 24) {
#pragma unroll
        for (int j = 0; j < 8; ++j) PIN(kg[j]);
        lds_st<u32x4>(LDS_KX + ch * KX_STRIDE + 512 + g * 16, u32x4{kg[0] | (kg[1] << 16), kg[2] | (kg[3] << 16), kg[4] | (kg[5] << 16), kg[6] | (g == 0 ? 0u : kg[7] << 16)});
      }
    });
    epi(3, [&](int k) { w2_rd(20 + k); });
  }
  STAMP(38);
  BAR();
  STAMP(6);
  rsrc_t rh = mk_rsrc(P.ws + WS_H, H_BYTES);
  const u32 hso = (u32)(((tt * 4 + th * 2) * 56 + kq * 14) * 1024);
  u32x4 hf[28];
  auto load_h = [&](int i) { hf[i] = ld16_async(rh, lane * 16, hso + ((i & 1) * 56 + (i >> 1)) * 1024); };
  const int trow = tt * 64 + th * 32 + lc, dcol = nb2 * 64 + 16 * kq + 4 * lg;
  u32x2 xres[2];
  u32x2 uu[2];
  {
    f32x4 ya[8];
#pragma unroll
    for (int T = 0; T < 8; ++T) ya[T] = f32x4{0.f, 0.f, 0.f, 0.f};
    const int s00 = 16 * (lg >> 1) + lc - 8 * (lg & 1) - 7;
    const int par = s00 & 1;
    const u32 abase = LDS_U + ch * U_CH + par * U_COPY + 32 + (s00 - par) * 2 + v4 * 128;
    const u32 bbase = LDS_KX + ch * KX_STRIDE + 512 + (16 * (lc - (lg >> 1)) + 8 * (lg & 1)) * 2 - v4 * 128 - 64;
    auto rdA = [&](int ph, int mu) {
      const u32 a = abase + ph * 64 + mu * 512;
      return u32x4{lds_ld<u32>(a), lds_ld<u32>(a + 4), lds_ld<u32>(a + 8), lds_ld<u32>(a + 12)};
    };
    const char* pfh = P.ws + WS_FH + (tt * 64 + 14 * kq) * FS;
    u64 fh = 0;
    u32x4 B[8], A[3];
    A[0] = rdA(0, 0);
#pragma unroll
    for (int g = 0; g < 8; ++g) B[g] = lds_ld<u32x4>(bbase + 64 + g * 512);
    A[1] = rdA(0, 1);
#pragma unroll
    for (int st = 0; st < 16; ++st) {
      const int ph = st >> 3, mu = st & 7;
      SCHED();
      if (st + 2 < 16) A[(st + 2) % 3] = rdA((st + 2) >> 3, (st + 2) & 7);
      SCHED();
#pragma unroll
      for (int T = 7; T >= mu; --T) {
        ya[T] = mma_h(A[st % 3], B[T - mu], ya[T]);
        if (T == 7 && ph == 0) { SCHED(); B[7 - mu] = lds_ld<u32x4>(bbase + (7 - mu) * 512); SCHED(); }
      }
      SCHED();
      if (st == PUBMU) {
        STAMP(39);
        WAIT_V(0);
        STAMP(40);
        if (!is_aux && lane == 0) st_agent(P.ws + WS_FH + ((mb * 2 + th) * 64 + nb * 4 + kq) * FS, tag);
      }
      if (st > PUBMU && st <= PUBMU + 6) {
        const int q = st - PUBMU - 1;
        w2_piece(rw2, vw2, sw2, 6 + 3 * (q / 3), 2 * (q % 3), (q / 3) * SLOT, lw2);
        w2_piece(rw2, vw2, sw2, 6 + 3 * (q / 3), 2 * (q % 3) + 1, (q / 3) * SLOT, lw2);
      }
      if (st == 11) {
#pragma unroll
        for (int t = 0; t < 2; ++t)
          asm volatile("buffer_load_dwordx2 %0, %1, %2, %3 offen" : "=v"(xres[t]) : "v"((u32)((trow * D + dcol) * 2)), "s"(rx), "s"(t * 16 * D * 2) : "memory");
#pragma unroll
        for (int i = 0; i < 2; ++i) uu[i] = lds_ld<u32x2>(LDS_U + ch * U_CH + 32 + (256 * (2 * v4 + i) + 16 * lc + 4 * lg) * 2);
      }
      if (st == 13) { BAR(); fh = ld8_async(pfh + (lane < 14 ? lane : 0) * FS); }
      if (st >= 13) {
#pragma unroll
        for (int T = (st == 13 ? 0 : mu - 1); T < mu; ++T) lds_st<f32x4>(SCR + (((ch * 4 + (T >> 1)) * 4 + v4) * 2 + (T & 1)) * 1024 + lane * 16, ya[T]);
      }
    }
    STAMP(7);
    lds_st<f32x4>(SCR + (((ch * 4 + 3) * 4 + v4) * 2 + 1) * 1024 + lane * 16, ya[7]);
    STAMP(41);
    WAIT_V(0);
    PIN(fh);
    if (!__all(fh == tag)) wait_flags(pfh, 14, tag, lane);
    STAMP(8);
#pragma unroll
    for (int i = 0; i < HW; ++i) load_h(i);
    BAR();
  }
  STAMP(9); CSTAMP(61);
  {
    f32x4 oa[4][2];
#pragma unroll
    for (int i = 0; i < 8; ++i) oa[i >> 1][i & 1] = f32x4{0.f, 0.f, 0.f, 0.f};
    rsrc_t ra = mk_rsrc(P.ws + WS_AO, AO_BYTES);
    const char* pfa = P.ws + WS_FA + ((tt >> 3) * 512 + nb2 * 64 + 16 * kq) * FS;
    u64 fa = 0;
    u32 ao[8];
    u32x4 cw[4];
    u32x2 nw[8];
    f32x4 y[4];
#pragma unroll
    for (int s = 0; s < 14; ++s) {
      if (HW + 2 * s < 28) { load_h(HW + 2 * s); load_h(HW + 2 * s + 1); }
      if (s < 12) asm volatile("s_waitcnt vmcnt(%2)" : "+v"(hf[2 * s]), "+v"(hf[2 * s + 1]) : "n"((int)((G2W >> (5 * s)) & 31)) : "memory");
      else { PIN(hf[2 * s]); PIN(hf[2 * s + 1]); }
      if (s == 0) STAMP(46);
      if (s == 3) BAR();
      if (s == 11) { STAMP(50); WAIT_V(1); BAR(); STAMP(51); }
#pragma unroll
      for (int i = 0; i < 8; ++i) {
        SCHED();
        oa[i >> 1][i & 1] = mma_bf(s < 6 ? wr[s < 6 ? s : 0][i >> 1] : cw[i >> 1], hf[2 * s + (i & 1)], oa[i >> 1][i & 1]);
        SCHED();
        if ((s == 1 || s == 2) && i == 1) {
          const int q = s - 1;
          const f32x4 ys = (y[0] + y[1]) + (y[2] + y[3]);
          float o[4];
#pragma unroll
          for (int r = 0; r < 4; ++r) {
            float u = h2f((uu[q][r >> 1] >> (16 * (r & 1))) & 0xffff);
            o[r] = fmaf(ys[r], kv.inv, u * kv.dsk);
          }
          u32x2 ov = {pk_bf16(o[0], o[1]), pk_bf16(o[2], o[3])};
          __builtin_amdgcn_raw_buffer_store_b64(ov, ra, (2 * b + ch) * AO_ROW + (256 * (2 * v4 + q) + 16 * lc + 4 * lg) * 2, 0, 16);
        }
        if (s < 2 && i >= 4) y[i - 4] = lds_ld<f32x4>(SCR + (((ch * 4 + v4) * 4 + i - 4) * 2 + s) * 1024 + lane * 16);
        if (s >= 3 && s <= 6 && i == 3) w2_piece(rw2, vw2, sw2, 12, s - 3, 2 * SLOT, lw2);
        if (s == 6 && i == 5) {
          WAIT_V(G2_AO);
          const char* p = P.ws + WS_FA + (v4 * 512 + 2 * b + ch) * FS;
          asm volatile("global_store_dwordx2 %0, %1, off sc1" :: "v"(p), "v"(tag) : "memory");
        }
        if (s == 10 && i == 0) fa = ld8_async(pfa + lc * FS);
        if (s == 12 && i == 7) {
          WAIT_V(0);
          PIN(fa);
          if (!__all(fa == tag)) wait_flags(pfa, 16, tag, lane);
#pragma unroll
          for (int e = 0; e < 8; ++e)
            asm volatile("buffer_load_ushort %0, %1, %2, %3 offen sc1" : "=v"(ao[e]) : "v"((u32)(dcol * AO_ROW + trow * 2)), "s"(ra), "s"((e & 3) * AO_ROW + (e >> 2) * 32) : "memory");
          STAMP(52);
        }
        if (s >= 5 && s < 13) {
          const int n = s + 1 - 6;
          nw[i] = lds_tr(w2a[i >> 1] + (n / 3) * SLOT + (n % 3) * 4096 + (i & 1) * 2048);
        }
      }
      SCHED();
#pragma unroll
      for (int g = 0; g < 4; ++g) cw[g] = cat(nw[2 * g], nw[2 * g + 1]);
    }
    STAMP(10);
#pragma unroll
    for (int g = 1; g < 4; ++g) {
      const u32 a = kq * 12288 + (th * 3 + g - 1) * 2048 + lane * 16;
      lds_st<f32x4>(a, oa[g][0]);
      lds_st<f32x4>(a + 1024, oa[g][1]);
    }
    WAIT_V(0);
    PIN(xres[0]); PIN(xres[1]);
#pragma unroll
    for (int e = 0; e < 8; ++e) {
      PIN(ao[e]);
      const u32 xw = xres[e >> 2][(e >> 1) & 1];
      oa[0][e >> 2][e & 3] += ((e & 1) ? bfhi(xw) : bf2f(xw)) + bf2f(ao[e]);
    }
    BAR();
#pragma unroll
    for (int g = 1; g < 4; ++g) {
      const u32 a = (kq ^ g) * 12288 + (th * 3 + g - 1) * 2048 + lane * 16;
      oa[0][0] += lds_ld<f32x4>(a); oa[0][1] += lds_ld<f32x4>(a + 1024);
    }
#pragma unroll
    for (int t = 0; t < 2; ++t)
      *(u32x2*)(P.out + (size_t)(trow + 16 * t) * D + dcol) = u32x2{pk_bf16(oa[0][t][0], oa[0][t][1]), pk_bf16(oa[0][t][2], oa[0][t][3])};
    STAMP(11); CSTAMP(62);
    STAMP_OUT;
    if (b == 255 && w == 0) {
#pragma unroll 1
      for (u32 sp = 0; sp < SPIN_CAP; ++sp) {
        u64 v0 = ld_agent(P.ws + WS_FA + lane * 2 * FS), v1 = ld_agent(P.ws + WS_FA + (128 + lane * 2) * FS);
        u64 v2 = ld_agent(P.ws + WS_FA + (256 + lane * 2) * FS), v3 = ld_agent(P.ws + WS_FA + (384 + lane * 2) * FS);
        if (__all(v0 == tag && v1 == tag && v2 == tag && v3 == tag)) break;
        __builtin_amdgcn_s_sleep(1);
      }
      if (tid == 0) st_agent(P.ws + WS_EP, tag);
    }
  }
}

extern "C" void kernel_launch(void* const* d_in, const int* in_sizes, int n_in,
                              void* d_out, int out_size, void* d_ws, size_t ws_size,
                              hipStream_t stream) {
  Params P;
  P.x = (const u16*)d_in[0]; P.lar = (const u16*)d_in[1]; P.aim = (const u16*)d_in[2];
  P.cre = (const u16*)d_in[3]; P.cim = (const u16*)d_in[4]; P.ldt = (const u16*)d_in[5];
  P.dsk = (const u16*)d_in[6]; P.w1 = (const u16*)d_in[8]; P.w3 = (const u16*)d_in[9];
  P.w2 = (const u16*)d_in[10]; P.out = (u16*)d_out; P.ws = (char*)d_ws;
  (void)hipFuncSetAttribute((const void*)fused, hipFuncAttributeMaxDynamicSharedMemorySize, LDS_TOTAL);
  fused<<<256, 512, LDS_TOTAL, stream>>>(P);
}
```

```cpp
#define SPINAT 11
#include <hip/hip_runtime.h>
#include <stdint.h>

typedef unsigned short u16;
typedef unsigned int u32;
typedef unsigned long long u64;
typedef __attribute__((ext_vector_type(4))) short s16x4;
typedef __attribute__((ext_vector_type(2))) u16 u16x2;
typedef __attribute__((ext_vector_type(8))) __bf16 bf16x8;
typedef __attribute__((ext_vector_type(8))) _Float16 f16x8;
typedef __attribute__((ext_vector_type(2))) __bf16 bf16x2;
typedef __attribute__((ext_vector_type(2))) _Float16 f16x2;
typedef __attribute__((ext_vector_type(4))) float f32x4;
typedef __attribute__((ext_vector_type(2))) float f32x2;
typedef __attribute__((ext_vector_type(4))) u32 u32x4;
typedef __attribute__((ext_vector_type(2))) u32 u32x2;
#define LAS __attribute__((address_space(3)))
#define GAS __attribute__((address_space(1)))
#define DEV __device__ __forceinline__
typedef __amdgpu_buffer_rsrc_t rsrc_t;

static constexpr int L = 2048, D = 512, NS = 128, F = 1792;

static constexpr size_t WS_H = 0;
static constexpr size_t WS_XT = 8u << 20;
static constexpr size_t WS_AO = 11u << 20;
static constexpr size_t WS_ST = 14u << 20;
static constexpr size_t WS_CS = WS_ST + (64u << 10);
static constexpr int FS = 128;
static constexpr size_t WS_FX = WS_ST + (128u << 10);
static constexpr size_t WS_FH = WS_FX + 32 * FS;
static constexpr size_t WS_FA = WS_FH + 2048 * FS;
static constexpr size_t WS_EP = WS_FA + 2048 * FS;
static constexpr int H_BYTES = L * F * 2;
static constexpr int XT_BLK = 65536 + 256, XT_BYTES = 32 * XT_BLK;
static constexpr int AO_ROW = 4096 + 128, AO_BYTES = D * AO_ROW;

static constexpr int SLOT = 49152;
static constexpr int LDS_TOTAL = 163840;
static constexpr int SCR = 2 * SLOT;
static constexpr int KG_ROW = 112, KG_PLANE = 128 * KG_ROW;
static constexpr int LDS_KD = SCR + 4 * KG_PLANE;
static constexpr int U_COPY = 4160;
static constexpr int U_CH = 2 * U_COPY;
static constexpr int LDS_U = SCR;
static constexpr int KX_STRIDE = 4608;
static constexpr int LDS_KX = SCR + 2 * U_CH;

struct Params {
  const u16 *x, *lar, *aim, *cre, *cim, *ldt, *dsk, *w1, *w3, *w2;
  u16* out;
  char* ws;
};

DEV float bf2f(u32 v) { return __uint_as_float(v << 16); }
DEV float bfhi(u32 v) { return __uint_as_float(v & 0xffff0000u); }
DEV u32 pk_bf16(float a, float b) { f32x2 p = {a, b}; return __builtin_bit_cast(u32, __builtin_convertvector(p, bf16x2)); }
DEV u32 pk_f16(float a, float b) { f32x2 p = {a, b}; return __builtin_bit_cast(u32, __builtin_convertvector(p, f16x2)); }
DEV float rbf(float a) { return bf2f(pk_bf16(a, a) & 0xffffu); }
DEV float h2f(u32 v) { return (float)__builtin_bit_cast(_Float16, (u16)v); }

template <typename T> DEV T lds_ld(u32 a) { return *(const LAS T*)a; }
template <typename T> DEV void lds_st(u32 a, T v) { *(LAS T*)a = v; }
DEV u32x2 lds_tr(u32 a) { return __builtin_bit_cast(u32x2, __builtin_amdgcn_ds_read_tr16_b64_v4i16((LAS s16x4*)a)); }
DEV u32x4 cat(u32x2 a, u32x2 b) { return __builtin_shufflevector(a, b, 0, 1, 2, 3); }
DEV f32x4 mma_bf(u32x4 a, u32x4 b, f32x4 c) { return __builtin_amdgcn_mfma_f32_16x16x32_bf16(__builtin_bit_cast(bf16x8, a), __builtin_bit_cast(bf16x8, b), c, 0, 0, 0); }
DEV f32x4 mma_h(u32x4 a, u32x4 b, f32x4 c) { return __builtin_amdgcn_mfma_f32_16x16x32_f16(__builtin_bit_cast(f16x8, a), __builtin_bit_cast(f16x8, b), c, 0, 0, 0); }
DEV rsrc_t mk_rsrc(const void* p, u32 bytes) { return __builtin_amdgcn_make_buffer_rsrc((void*)p, 0, bytes, 0x00020000); }
DEV void dma16(rsrc_t rs, u32 voff, u32 sbase, int simm, u32 lbase, int limm) {
  u32 t;
  asm volatile("s_add_u32 m0, %1, %2\n\ts_add_u32 %0, %3, %4\n\tbuffer_load_dwordx4 %5, %6, %0 offen lds"
               : "=&s"(t) : "s"(lbase), "n"(limm), "s"(sbase), "n"(simm), "v"(voff), "s"(rs) : "memory", "scc");
}
#define PINS(x) asm volatile("" : "+s"(x))
#define WAIT_V(n) asm volatile("s_waitcnt vmcnt(%0)" :: "n"(n) : "memory")
#define BAR() asm volatile("s_waitcnt lgkmcnt(0)\n\ts_barrier" ::: "memory")
#define SCHED() __builtin_amdgcn_sched_barrier(0)
#define PIN(x) asm volatile("" : "+v"(x))
#define RLX __ATOMIC_RELAXED, __HIP_MEMORY_SCOPE_AGENT
DEV u64 ld_agent(const char* p) { return __hip_atomic_load((GAS u64*)p, RLX); }
DEV void st_agent(char* p, u64 v) { __hip_atomic_store((GAS u64*)p, v, RLX); }
#define PUBMU 3
static constexpr u32 SPIN_CAP = 1u << 16;

#define STAMP(i) do { if ((i) == SPINAT) { while (__builtin_amdgcn_s_memrealtime() - stv < 3000ull) __builtin_amdgcn_s_sleep(2); } } while (0)
#define STAMP_DECL u64 stv = __builtin_amdgcn_s_memrealtime()
#define CSTAMP(i)
#define STAMP_OUT
#define STV_ARG , u64 stv
#define STV_PASS , stv

DEV void wait_flags(const char* base, int n, u64 tag, int lane) {
  const char* p = base + (lane < n ? lane : 0) * FS;
#pragma unroll 1
  for (u32 s = 0; s < SPIN_CAP; ++s) {
    u64 v = ld_agent(p);
    if (__all(v == tag)) break;
    __builtin_amdgcn_s_sleep(1);
  }
  asm volatile("" ::: "memory");
}
DEV u64 ld8_async(const char* p) {
  u64 v;
  asm volatile("global_load_dwordx2 %0, %1, off sc1" : "=v"(v) : "v"(p) : "memory");
  return v;
}
DEV u32x4 ld16_async(rsrc_t rs, u32 voff, u32 soff) {
  u32x4 v;
  asm volatile("buffer_load_dwordx4 %0, %1, %2, %3 offen sc1" : "=v"(v) : "v"(voff), "s"(rs), "s"(soff) : "memory");
  return v;
}
DEV u32 ld_u16_async(const u16* base, u32 off) {
  u32 v;
  asm volatile("global_load_ushort %0, %1, %2" : "=v"(v) : "v"(off), "s"(base) : "memory");
  return v;
}
template <int C> DEV float dppf(float v) { return __int_as_float(__builtin_amdgcn_update_dpp(0, __float_as_int(v), C, 0xf, 0xf, true)); }
DEV float row_sum(float v) { v += dppf<0x128>(v); v += dppf<0x124>(v); v += dppf<0x122>(v); v += dppf<0x121>(v); return v; }
DEV float row_max(float v) { v = fmaxf(v, dppf<0x128>(v)); v = fmaxf(v, dppf<0x124>(v)); v = fmaxf(v, dppf<0x122>(v)); v = fmaxf(v, dppf<0x121>(v)); return v; }
DEV float fold32(float a, float b) {
  auto r = __builtin_amdgcn_permlane32_swap(__float_as_uint(a), __float_as_uint(b), false, false);
  return __uint_as_float(r[0]) + __uint_as_float(r[1]);
}
DEV float fold16(float a, float b) {
  auto r = __builtin_amdgcn_permlane16_swap(__float_as_uint(a), __float_as_uint(b), false, false);
  return __uint_as_float(r[0]) + __uint_as_float(r[1]);
}
DEV float dot2bf(u32 a, u32 b, float c) { return __builtin_amdgcn_fdot2_f32_bf16(__builtin_bit_cast(bf16x2, a), __builtin_bit_cast(bf16x2, b), c, false); }
DEV float rdl(float v, int l) { return __int_as_float(__builtin_amdgcn_readlane(__float_as_int(v), l)); }
DEV u64 mk_tag(u64 ep) {
  u32 lo, hi;
  asm volatile("v_readfirstlane_b32 %0, %2\n\tv_readfirstlane_b32 %1, %3" : "=s"(lo), "=s"(hi) : "v"((u32)ep), "v"((u32)(ep >> 32)) : "memory");
  return (((u64)hi << 32) | lo) * 6364136223846793005ull + 1442695040888963407ull;
}

DEV u64 aux_phase(const Params& P, int a, u64 ep, int w, int lane STV_ARG) {
  const int tid = threadIdx.x;
  u32x4 xv[8];
#pragma unroll
  for (int i = 0; i < 8; ++i) xv[i] = *(const u32x4*)(P.x + (size_t)(64 * a + 8 * w + i) * D + lane * 8);
  u32x4 wv[16];
  const int ca = a < 28 ? a : 27;
  const int cmat = ca / 14, cnb = ca % 14;
  rsrc_t rw = mk_rsrc(cmat == 1 ? P.w3 : P.w1, D * F * 2);
  const u32 vo = (tid >> 4) * (F * 2) + cnb * 256 + (tid & 15) * 16;
  WAIT_V(8);
  const u64 tag = mk_tag(ep);
  STAMP(16);
  float s1[8], s2[8];
#pragma unroll
  for (int i = 0; i < 8; ++i) {
    s1[i] = 0.f; s2[i] = 0.f;
#pragma unroll
    for (int j = 0; j < 4; ++j) { s1[i] = dot2bf(xv[i][j], 0x3f803f80u, s1[i]); s2[i] = dot2bf(xv[i][j], xv[i][j], s2[i]); }
    wv[i] = __builtin_amdgcn_raw_buffer_load_b128(rw, vo, i * 32 * F * 2, 0);
    SCHED();
  }
  float rs[2], mr[2];
#pragma unroll
  for (int g = 0; g < 2; ++g) {
    float S = row_sum(fold16(fold32(s1[4 * g], s1[4 * g + 2]), fold32(s1[4 * g + 1], s1[4 * g + 3])));
    float Q = row_sum(fold16(fold32(s2[4 * g], s2[4 * g + 2]), fold32(s2[4 * g + 1], s2[4 * g + 3])));
    float mf = S * (1.f / D);
    float var = rbf(fmaxf(Q * (1.f / D) - mf * mf, 0.f));
    float m = rbf(mf);
    float sd = rbf(__builtin_amdgcn_sqrtf(rbf(var + 1e-5f)));
    rs[g] = __builtin_amdgcn_rcpf(sd);
    mr[g] = -m * rs[g];
    if ((lane & 15) == 0)
      st_agent(P.ws + WS_ST + (size_t)(64 * a + 8 * w + 4 * g + (lane >> 4)) * 8,
               ((u64)(u32)tag << 32) | (__float_as_uint(sd) & 0xffff0000u) | (__float_as_uint(m) >> 16));
  }
  STAMP(17);
  float rr[8], cc[8];
#pragma unroll
  for (int i = 0; i < 8; ++i) { rr[i] = rdl(rs[i >> 2], (i & 3) * 16); cc[i] = rdl(mr[i >> 2], (i & 3) * 16); }
#pragma unroll
  for (int e = 0; e < 8; ++e) {
    u32x4 o;
#pragma unroll
    for (int j = 0; j < 4; ++j) {
      const u32 v0 = xv[2 * j][e >> 1], v1 = xv[2 * j + 1][e >> 1];
      o[j] = pk_f16(fmaf((e & 1) ? bfhi(v0) : bf2f(v0), rr[2 * j], cc[2 * j]), fmaf((e & 1) ? bfhi(v1) : bf2f(v1), rr[2 * j + 1], cc[2 * j + 1]));
    }
    lds_st<u32x4>((u32)((8 * lane + e) * 128 + ((w ^ (lane & 7)) * 16)), o);
    wv[8 + e] = __builtin_amdgcn_raw_buffer_load_b128(rw, vo, (8 + e) * 32 * F * 2, 0);
    SCHED();
  }
  STAMP(18);
  BAR();
  STAMP(19);
  rsrc_t rx = mk_rsrc(P.ws + WS_XT, XT_BYTES);
#pragma unroll
  for (int j = 0; j < 8; ++j) {
    int d = (tid >> 3) + 64 * j, s = tid & 7;
    u32x4 o = lds_ld<u32x4>((u32)(d * 128 + ((s ^ ((d >> 3) & 7)) * 16)));
    __builtin_amdgcn_raw_buffer_store_b128(o, rx, a * XT_BLK + d * 128 + s * 16, 0, 16);
  }
  STAMP(20);
  {
    u32x4 sel;
    const int i = lane & 15;
    const u32 one = (i & 1) ? 0x3f800000u : 0x3f80u;
    sel[0] = (i >> 1) == 0 ? one : 0; sel[1] = (i >> 1) == 1 ? one : 0;
    sel[2] = (i >> 1) == 2 ? one : 0; sel[3] = (i >> 1) == 3 ? one : 0;
    f32x4 c0 = {0.f, 0.f, 0.f, 0.f}, c1 = c0;
#pragma unroll
    for (int k = 0; k < 16; k += 2) { c0 = mma_bf(sel, wv[k], c0); c1 = mma_bf(sel, wv[k + 1], c1); }
    if (lane < 32) lds_st<f32x4>(65536 + w * 512 + (lane & 15) * 32 + (lane >> 4) * 16, c0 + c1);
  }
  STAMP(21);
  WAIT_V(0);
  STAMP(22);
  BAR();
  if (tid == 0) st_agent(P.ws + WS_FX + a * FS, tag);
  if (tid < 128) {
    float s = 0.f;
#pragma unroll
    for (int j = 0; j < 8; ++j) s += lds_ld<float>(65536 + j * 512 + tid * 4);
    st_agent(P.ws + WS_CS + (size_t)(cmat * F + cnb * 128 + tid) * 8, ((u64)(u32)tag << 32) | __float_as_uint(s));
  }
  BAR();
  return tag;
}

struct cpx { float r, i; };
struct cpx2 { f32x2 r, i; };
DEV cpx cmul(cpx a, cpx b) { return cpx{fmaf(a.r, b.r, -(a.i * b.i)), fmaf(a.r, b.i, a.i * b.r)}; }
DEV cpx csqr(cpx a) { return cpx{fmaf(a.r, a.r, -(a.i * a.i)), (a.r + a.r) * a.i}; }
DEV cpx2 cmul2(cpx2 a, cpx b) {
  f32x2 br = {b.r, b.r}, bi = {b.i, b.i};
  return cpx2{__builtin_elementwise_fma(a.r, br, -(a.i * bi)), __builtin_elementwise_fma(a.r, bi, a.i * br)};
}
DEV cpx2 cpair(cpx a, cpx b) { return cpx2{f32x2{a.r, b.r}, f32x2{a.i, b.i}}; }
DEV cpx hi_of(cpx2 a) { return cpx{a.r[1], a.i[1]}; }
DEV cpx cexp_at(float ar2, float airev, float p) {
  float e = __builtin_amdgcn_exp2f(ar2 * p);
  float fr = __builtin_amdgcn_fractf(airev * p);
  return cpx{e * __builtin_amdgcn_cosf(fr), e * __builtin_amdgcn_sinf(fr)};
}
struct KgIn { u32 lar, aim, cr, ci, cr2, ci2, ldt, dsk; };
struct KgVal { u32 re[16], im[16]; float inv, dsk; };
DEV KgIn kgen_load(const Params& P, int c, int w, int lane) {
  const int d = 2 * c + (w >> 2);
  const u32 o = (d * NS + 64 * ((w >> 1) & 1) + lane) * 2, o2 = o ^ 128, od = d * 2;
  KgIn k;
  k.lar = ld_u16_async(P.lar, o); k.aim = ld_u16_async(P.aim, o);
  k.cr = ld_u16_async(P.cre, o); k.ci = ld_u16_async(P.cim, o);
  k.cr2 = ld_u16_async(P.cre, o2); k.ci2 = ld_u16_async(P.cim, o2);
  k.ldt = ld_u16_async(P.ldt, od); k.dsk = ld_u16_async(P.dsk, od);
  return k;
}
template <typename FN> DEV KgVal kgen_values(KgIn k, int w, FN&& side) {
  asm volatile("" : "+v"(k.lar), "+v"(k.aim), "+v"(k.cr), "+v"(k.ci), "+v"(k.cr2), "+v"(k.ci2), "+v"(k.ldt), "+v"(k.dsk));
  KgVal o;
  float cr = bf2f(k.cr), ci = bf2f(k.ci), cr2 = bf2f(k.cr2);
  float dt = rbf(__expf(bf2f(k.ldt)));
  float ar = -rbf(__expf(bf2f(k.lar))) * dt, ai = bf2f(k.aim) * dt;
  float ar2 = ar * 1.44269504088896f, airev = ai * 0.159154943091895f;
  side(0);
  float mx = row_max(fmaxf(fabsf(cr) + fabsf(ci), fabsf(cr2) + fabsf(bf2f(k.ci2))));
  float sm = row_sum(cr + cr2);
  o.dsk = bf2f(k.dsk) + 2.f * dt * ((rdl(sm, 0) + rdl(sm, 16)) + (rdl(sm, 32) + rdl(sm, 48)));
  side(1);
  u32 mb = max(max((u32)__builtin_amdgcn_readlane(__float_as_int(mx), 0), (u32)__builtin_amdgcn_readlane(__float_as_int(mx), 16)),
               max((u32)__builtin_amdgcn_readlane(__float_as_int(mx), 32), (u32)__builtin_amdgcn_readlane(__float_as_int(mx), 48)));
  float M = 2.f * dt * __uint_as_float(mb);
  int sexp = 261 - (int)((__float_as_uint(M) >> 23) & 0xff);
  sexp = sexp > 254 ? 254 : sexp;
  o.inv = __uint_as_float((u32)(254 - sexp) << 23);
  float cs = 2.f * dt * __uint_as_float((u32)sexp << 23);
  cpx C{cr * cs, ci * cs};
  side(2);
  cpx w128 = cexp_at(ar2, airev, 128.f), w256 = csqr(w128);
  side(3);
  cpx2 V[16];
  if (w & 1) {
    cpx w1 = cexp_at(ar2, airev, 1.f), w2 = csqr(w1), w4 = csqr(w2), w512 = csqr(w256);
    V[0] = cmul2(cpair(w1, w2), C);
    V[1] = cmul2(V[0], w2); V[2] = cmul2(V[0], w4); V[3] = cmul2(V[1], w4);
#pragma unroll
    for (int r = 0; r < 4; ++r) V[4 + r] = cmul2(V[r], w128);
    V[8] = cmul2(cpair(hi_of(V[0]), hi_of(V[1])), w256);
    V[9] = cmul2(V[8], w4);
    V[10] = cmul2(V[8], w128); V[11] = cmul2(V[9], w128);
    V[12] = cmul2(cpair(hi_of(V[1]), hi_of(V[3])), w512);
    V[13] = cmul2(V[12], w128); V[14] = cmul2(V[12], w256); V[15] = cmul2(V[13], w256);
  } else {
    cpx g = cmul(C, cexp_at(ar2, airev, 1032.f));
    V[0] = cpair(g, cmul(g, w128));
#pragma unroll
    for (int r = 1; r < 4; ++r) V[r] = cmul2(V[r - 1], w256);
    cpx y8 = cexp_at(ar2, -airev, 8.f), y16 = csqr(y8), y32 = csqr(y16), y64 = csqr(y32);
    V[4] = cpair(cpx{1.f, 0.f}, y8);
    V[5] = cmul2(V[4], y16); V[6] = cmul2(V[4], y32); V[7] = cmul2(V[5], y32);
#pragma unroll
    for (int r = 0; r < 4; ++r) V[8 + r] = cmul2(V[4 + r], y64);
#pragma unroll
    for (int r = 12; r < 16; ++r) V[r] = V[r - 12];
  }
#pragma unroll
  for (int r = 0; r < 16; ++r) { if ((r & 7) == 0) side(4 + (r >> 3)); o.re[r] = pk_f16(V[r].r[0], V[r].r[1]); o.im[r] = pk_f16(V[r].i[0], V[r].i[1]); }
  return o;
}
DEV void kgen_store(const KgVal& v, int c, u32 base) {
  lds_st<u32x4>(base, u32x4{v.re[4 * c], v.re[4 * c + 1], v.re[4 * c + 2], v.re[4 * c + 3]});
  lds_st<u32x4>(base + KG_PLANE, u32x4{v.im[4 * c], v.im[4 * c + 1], v.im[4 * c + 2], v.im[4 * c + 3]});
}
struct KdTab { u16 v[2048]; };
constexpr KdTab make_kd() {
  KdTab t{};
  for (int i = 0; i < 2048; ++i) {
    int l = (i & ~7) + 7 - (i & 7);
    if (l == 0) l = 1;
    int sh = l < 256 ? 0 : l < 512 ? 1 : l < 1024 ? 2 : 3;
    int q = l >> sh, rem = l - (q << sh), half = (1 << sh) >> 1;
    if (sh > 0 && (rem > half || (rem == half && (q & 1)))) ++q;
    int p = (q << sh) - 1;
    int I = p >> 7, sg = I < 2 ? 0 : I < 4 ? 1 : I < 8 ? 2 : 3;
    int r = 8 * sg + (I << (3 - sg)) + ((p & 7) >> sg);
    t.v[i] = (u16)((r * 16 + ((p >> 3) & 15)) * 2);
  }
  return t;
}
__constant__ __attribute__((aligned(16))) KdTab kd_tab = make_kd();

DEV void w2_piece(rsrc_t rw2, u32 vw2, u32 sw2, int k0, int i, int slot, u32 lw2) {
  dma16(rw2, vw2, sw2, (k0 * 2 + i) * 16384, lw2, slot + i * 2048);
}

#define HW 10
constexpr int g2_ops_at(int s) { return (HW + 2 * s < 28 ? 2 : 0) + (s == 1 || s == 2 ? 1 : 0) + (s >= 3 && s <= 6 ? 1 : 0) + (s == 6 ? 1 : 0) + (s == 10 ? 1 : 0) + (s == 12 ? 8 : 0); }
constexpr int g2_issued_through(int s) { int n = HW; for (int i = 0; i <= s; ++i) n += g2_ops_at(i); return n; }
constexpr int g2_pos_hf(int i) { return i < HW ? i + 1 : g2_issued_through((i - HW) / 2 - 1) + 1 + ((i - HW) & 1); }
constexpr int g2_wait_hf(int i, int s) { return g2_issued_through(s - 1) + (HW + 2 * s < 28 ? 2 : 0) - g2_pos_hf(i); }
constexpr u64 g2_pack() { u64 v = 0; for (int s = 0; s < 12; ++s) v |= (u64)g2_wait_hf(2 * s + 1, s) << (5 * s); return v; }
static constexpr u64 G2W = g2_pack();
static constexpr int G2_AO = g2_issued_through(6) - 1 - g2_issued_through(2);

__global__ __launch_bounds__(512, 2) void fused(Params P) {
  const int b = blockIdx.x, xcd = b & 7, idx = b >> 3;
  const int tid = threadIdx.x, lane = tid & 63;
  const int w = __builtin_amdgcn_readfirstlane(tid >> 6);
  STAMP_DECL;
  STAMP(0); CSTAMP(58);
  KgIn kin = kgen_load(P, b, w, lane);
  u64 ep;
  asm volatile("global_load_dwordx2 %0, %1, %2 sc1" : "=v"(ep) : "v"((u32)WS_EP), "s"(P.ws) : "memory");
  STAMP(12);
  const bool is_aux = idx >= 28;
  const int mb = (xcd & 3) * 4 + (idx & 3), nb = (xcd >> 2) * 7 + (idx >> 2);
  const int tt = xcd * 4 + (idx & 3), nb2 = idx >> 2;
  const int th = w & 1, kq = w >> 1;
  const int ch = w >> 2, v4 = w & 3;
  const int lc = lane & 15, lg = lane >> 4, lq = (lane & 15) >> 2, lp = lane & 3;
  rsrc_t rx = mk_rsrc(P.x, L * D * 2);
  rsrc_t rxg = mk_rsrc(P.x, is_aux ? 0 : L * D * 2);
  rsrc_t rw = mk_rsrc(w >= 4 ? P.w3 : P.w1, is_aux ? 0 : D * F * 2);
  const u32 vx0 = (lane >> 3) * 1024 + (((lane & 7) ^ lg) * 16);
  const u32 vx1 = vx0 ^ 64;
  const u32 vw0 = lg * (F * 2) + ((lc ^ (lg << 1)) * 16);
  const u32 vw1 = vw0 ^ 128;
  const u32 sx0 = (u32)((mb * 128 + 16 * w) * 1024), sw0 = (u32)(16 * (w & 3) * (F * 2) + nb * 256);
  const u32 lx0 = w * 2048, lw0 = w * 4096;
  auto g1_piece = [&](int s, int i) {
    const int slot = ((s + 1) % 3) * SLOT;
    if (i == 0) dma16(rxg, vx0, sx0, s * 128, lx0, slot);
    else if (i == 1) dma16(rxg, vx1, sx0, s * 128 + 8192, lx0, slot + 1024);
    else dma16(rw, i < 4 ? vw0 : vw1, sw0, (s * 64 + (i - 2) * 4) * F * 2, lw0, slot + 16384 + (i - 2) * 1024);
  };

  u64 tag;
  if (__builtin_expect(!is_aux, 1)) {
#pragma unroll
    for (int i = 0; i < 6; ++i) g1_piece(0, i);
    STAMP(13);
    WAIT_V(6);
    tag = mk_tag(ep);
  } else {
    tag = aux_phase(P, xcd * 4 + idx - 28, ep, w, lane STV_PASS);
  }
  STAMP(1);
  KgVal kv = kgen_values(kin, w, [&](int i) { g1_piece(1, i); });
  STAMP(2); CSTAMP(59);
  rsrc_t rw2 = mk_rsrc(P.w2, F * D * 2);
  const u32 vw2 = (lane >> 3) * 1024 + (((lane & 7) ^ (lg << 1)) * 16);
  u32 sw2 = (u32)((kq * 448 + th * 8) * 1024 + nb2 * 128), lw2 = kq * 12288 + th * 1024;
  PINS(sw2); PINS(lw2);
  u32x4 uv, kdv; u32 unx;
  auto load_u = [&]() {
    rsrc_t rxt = mk_rsrc(P.ws + WS_XT, XT_BYTES);
    const int g = tid & 255, gn = g + 1;
    const u32 cb = (2 * b + ch) * 128;
    uv = ld16_async(rxt, (g >> 3) * XT_BLK + (g & 7) * 16 + cb, 0);
    asm volatile("buffer_load_dword %0, %1, %2, 0 offen sc1" : "=v"(unx) : "v"((gn >> 3) * XT_BLK + (gn & 7) * 16 + cb), "s"(rxt) : "memory");
    asm volatile("global_load_dwordx4 %0, %1, %2" : "=v"(kdv) : "v"(g * 16), "s"(&kd_tab) : "memory");
  };
  auto kt_store = [&](int c) {
    const u32 kbase = SCR + ch * 2 * KG_PLANE + (64 * ((w >> 1) & 1) + lane) * KG_ROW;
    kgen_store(kv, c, kbase + (c < 3 ? ((w & 1) ^ 1) * 64 + c * 16 : 64 - (w & 1) * 16));
  };

  f32x4 acc[4][4];
#pragma unroll
  for (int i = 0; i < 16; ++i) acc[i >> 2][i & 3] = f32x4{0.f, 0.f, 0.f, 0.f};
  u32 sdv[4] = {0x3f800000u, 0x3f800000u, 0x3f800000u, 0x3f800000u};

  if (__builtin_expect(!is_aux, 1)) {
    u32 xa[2], wa[2];
    {
      const int row = 64 * th + lc;
      xa[0] = SLOT + row * 128 + ((lg ^ ((row >> 1) & 7)) * 16);
      xa[1] = xa[0] ^ 64;
      wa[0] = SLOT + 16384 + (8 * lg + lq) * 256 + (((2 * kq) ^ (lq | ((lg & 1) << 2))) * 32) + lp * 8;
      wa[1] = wa[0] ^ 32;
    }
    const char* pcs = P.ws + WS_CS + (size_t)((lg >> 1) * F + nb * 128 + kq * 32 + (lg & 1) * 16 + lc) * 8;
    const char* pst = P.ws + WS_ST + (size_t)(mb * 128 + 64 * th + lc) * 8;
    u64 gc, gs[4], fx;
    const char* pfx = P.ws + WS_FX + (4 * w + lp) * FS;
    WAIT_V(6);
    BAR();
    STAMP(3);
    u32x4 cx[4], cw[4], nx[4];
    u32x2 nw[8];
    auto rd = [&](int i, int ks) {
      if (i < 4) nx[i] = lds_ld<u32x4>(xa[ks] + i * 2048);
      else nw[i - 4] = lds_tr(wa[((i - 4) >> 1) & 1] + ((i - 4) >> 2) * 16384 + ks * 8192 + ((i - 4) & 1) * 1024);
    };
    auto rot = [&]() {
#pragma unroll
      for (int i = 0; i < 4; ++i) { cx[i] = nx[i]; cw[i] = cat(nw[2 * i], nw[2 * i + 1]); }
    };
#pragma unroll
    for (int i = 0; i < 12; ++i) rd(i, 0);
#pragma unroll
    for (int i = 0; i < 3; ++i) g1_piece(2, i);
    rot();
#pragma unroll
    for (int s = 0; s < 8; ++s) {
#pragma unroll
      for (int ks = 0; ks < 2; ++ks) {
        if (ks == 1) {
          if (s >= 5) STAMP(16 + 2 * s);
          WAIT_V(6);
          if (s == 7) {
            const u32 t32 = (u32)tag;
            PIN(fx); PIN(gc); PIN(gs[0]); PIN(gs[1]); PIN(gs[2]); PIN(gs[3]);
            auto ok = [&]() {
              return __all((((u32)(gc >> 32) ^ t32) | ((u32)(gs[0] >> 32) ^ t32) | ((u32)(gs[1] >> 32) ^ t32) | ((u32)(gs[2] >> 32) ^ t32) |
                            ((u32)(gs[3] >> 32) ^ t32) | ((u32)fx ^ t32) | ((u32)(fx >> 32) ^ (u32)(tag >> 32))) == 0);
            };
            if (__builtin_expect(!ok(), 0)) {
#pragma unroll 1
              for (u32 sp = 0; sp < SPIN_CAP; ++sp) {
                gc = ld_agent(pcs); fx = ld_agent(pfx);
#pragma unroll
                for (int t = 0; t < 4; ++t) gs[t] = ld_agent(pst + t * 128);
                if (ok()) break;
                __builtin_amdgcn_s_sleep(1);
              }
            }
          }
          BAR();
          if (s < 7) {
            const int dl = (s + 2) % 3 == 0 ? -2 * SLOT : SLOT;
            xa[0] += dl; xa[1] += dl; wa[0] += dl; wa[1] += dl;
            PIN(xa[0]); PIN(xa[1]); PIN(wa[0]); PIN(wa[1]);
          }
          if (s >= 5) STAMP(17 + 2 * s);
        }
        const int rs = ks == 1 ? s : s - 1;
#pragma unroll
        for (int i = 0; i < 16; ++i) {
          SCHED();
          acc[i >> 2][i & 3] = mma_bf(cw[i >> 2], cx[i & 3], acc[i >> 2][i & 3]);
          SCHED();
          static constexpr int order[12] = {0, 4, 5, 1, 2, 3, 6, 7, 8, 9, 10, 11};
          if (i < 12 && !(s == 7 && ks == 1)) rd(order[i], ks ^ 1);
          if ((i == 2 || i == 7 || i == 12)) {
            const int pi = (ks == 1 ? 0 : 3) + i / 5;
            if (rs + 3 < 8) g1_piece(rs + 3, pi);
            else if (rs == 5) w2_piece(rw2, vw2, sw2, 0, pi, 0, lw2);
            else if (rs == 6) w2_piece(rw2, vw2, sw2, 3, pi, SLOT, lw2);
          }
          if (s == 6 && ks == 1 && i == 0) {
            gc = ld8_async(pcs);
#pragma unroll
            for (int t = 0; t < 4; ++t) gs[t] = ld8_async(pst + t * 128);
            fx = ld8_async(pfx);
          }
          if (s == 7 && ks == 1) {
            if (i == 0) load_u();
            if ((i & 3) == 2) kt_store(i >> 2);
          }
        }
        SCHED();
        rot();
      }
    }
    STAMP(4);
    const float cv = __uint_as_float((u32)gc);
    const float c0 = rbf(cv), r1 = cv - c0, c1 = rbf(r1);
    const u32 e0 = pk_bf16(c0, c1), e1 = pk_bf16(r1 - c1, 0.f);
#pragma unroll
    for (int t = 0; t < 4; ++t) {
      const u32 m = ((u32)gs[t] & 0xffffu) ^ 0x8000u;
      const u32x4 mv = {m | (m << 16), m, 0, 0};
#pragma unroll
      for (int a = 0; a < 4; ++a) acc[a][t] = mma_bf(u32x4{lg == a ? e0 : 0u, lg == a ? e1 : 0u, 0, 0}, mv, acc[a][t]);
      sdv[t] = (u32)gs[t] & 0xffff0000u;
    }
  } else {
    WAIT_V(0);
    BAR();
#pragma unroll
    for (int i = 0; i < 6; ++i) w2_piece(rw2, vw2, sw2, 0, i, 0, lw2);
#pragma unroll
    for (int i = 0; i < 6; ++i) w2_piece(rw2, vw2, sw2, 3, i, SLOT, lw2);
#pragma unroll
    for (int i = 0; i < 4; ++i) kt_store(i);
    wait_flags(P.ws + WS_FX + 4 * w * FS, 4, tag, lane);
    BAR();
    load_u();
    WAIT_V(0);
  }
  STAMP(5); CSTAMP(60);
  BAR();
  STAMP(32);
  u32 w2a[4];
#pragma unroll
  for (int g = 0; g < 4; ++g) w2a[g] = kq * 12288 + (4 * lg + lq) * 128 + (((g ^ kq) ^ ((2 * lg + (lq >> 1)) & 3)) * 32) + lp * 8;
  u32x4 wr[6][4];
  auto w2_frag = [&](u32 off, int g) { return cat(lds_tr(w2a[g] + off), lds_tr(w2a[g] + off + 2048)); };
  u32x2 wh[6][8];
  auto w2_rd = [&](int i) {
    const int t = i >> 3, g = (i >> 1) & 3;
    wh[t][i & 7] = lds_tr(w2a[g] + (t / 3) * SLOT + (t % 3) * 4096 + (i & 1) * 2048);
    if (i & 1) wr[t][g] = cat(wh[t][(i & 7) - 1], wh[t][i & 7]);
  };
  {
    rsrc_t rhs = mk_rsrc(P.ws + WS_H, is_aux ? 0 : H_BYTES);
    const u32 ho = (u32)(((mb * 8 + th * 4) * 56 + nb * 4 + kq) * 1024 + lane * 16);
    auto epi = [&](int t, auto&& side) {
      const float sd = __uint_as_float(sdv[t]);
      float ke = -1.44269504088896f * __builtin_amdgcn_rcpf(sd), s2 = sd * sd;
      asm("" : "+v"(s2), "+v"(ke));
      f32x2 e[4], gu[4];
#pragma unroll
      for (int j = 0; j < 4; ++j) {
        const f32x2 g = {acc[j >> 1][t][2 * (j & 1)], acc[j >> 1][t][2 * (j & 1) + 1]};
        const f32x2 u = {acc[2 + (j >> 1)][t][2 * (j & 1)], acc[2 + (j >> 1)][t][2 * (j & 1) + 1]};
        e[j] = g * ke;
        gu[j] = g * u;
        SCHED(); side(j); SCHED();
      }
#pragma unroll
      for (int j = 0; j < 8; ++j) { e[j >> 1][j & 1] = __builtin_amdgcn_exp2f(e[j >> 1][j & 1]); SCHED(); side(4 + j); SCHED(); }
#pragma unroll
      for (int j = 0; j < 4; ++j) { e[j] = __builtin_elementwise_fma(e[j], f32x2{s2, s2}, f32x2{s2, s2}); SCHED(); side(12 + j); SCHED(); }
#pragma unroll
      for (int j = 0; j < 8; ++j) { e[j >> 1][j & 1] = __builtin_amdgcn_rcpf(e[j >> 1][j & 1]); SCHED(); side(16 + j); SCHED(); }
      u32x4 o;
#pragma unroll
      for (int j = 0; j < 4; ++j) { gu[j] *= e[j]; SCHED(); side(24 + j); SCHED(); }
#pragma unroll
      for (int j = 0; j < 4; ++j) o[j] = pk_bf16(gu[j][0], gu[j][1]);
      __builtin_amdgcn_raw_buffer_store_b128(o, rhs, ho + t * 56 * 1024, 0, 16);
      SCHED();
    };
    const int kt = v4 < 3 ? v4 : 0;
    const u32 krb = SCR + ch * 2 * KG_PLANE + (8 * lg + lq) * KG_ROW + lp * 8, kra = krb + kt * 32;
    u32x2 kf[8][4];
    f32x4 ka = {0.f, 0.f, 0.f, 0.f}, kb = ka;
    auto kt_rd = [&](int i) { kf[i >> 2][i & 3] = lds_tr(((i & 2) ? krb + 80 : kra) + (i >> 2) * 32 * KG_ROW + (i & 1) * 4 * KG_ROW); };
    auto kt_mma = [&](int k) {
      if (k & 1) kb = mma_h(cat(kf[k][2], kf[k][3]), cat(kf[k][0], kf[k][1]), kb);
      else ka = mma_h(cat(kf[k][2], kf[k][3]), cat(kf[k][0], kf[k][1]), ka);
    };
#pragma unroll
    for (int i = 0; i < 4; ++i) kt_rd(i);
    SCHED();
    epi(0, [&](int k) { kt_rd(4 + k); if (k >= 8 && (k & 3) == 1) kt_mma((k - 8) >> 2); });
    epi(1, [&](int k) {
      if (k == 5 || k == 7 || k == 9) kt_mma(5 + ((k - 5) >> 1));
      if (k == 20) {
        ka += kb;
        lds_st<u32x2>(LDS_KD + ch * 1536 + ((16 * kt + lc) * 16 + 4 * lg) * 2, u32x2{pk_f16(ka[0], ka[1]), pk_f16(ka[2], ka[3])});
      }
      if (k < 20) w2_rd(k);
    });
    STAMP(34);
    WAIT_V(2);
    BAR();
    STAMP(35);
    PIN(uv); PIN(unx); PIN(kdv);
    const int g = tid & 255;
    u32 kg[8];
    epi(2, [&](int k) {
      if (k < 8) kg[k] = lds_ld<u16>(LDS_KD + ch * 1536 + ((k & 1) ? kdv[k >> 1] >> 16 : kdv[k >> 1] & 0xffffu));
      if (k == 9) lds_st<u32x4>(LDS_U + ch * U_CH + 32 + g * 16, uv);
      if (k == 14) lds_st<u32x4>(LDS_U + ch * U_CH + U_COPY + 32 + g * 16,
                                 u32x4{__builtin_amdgcn_alignbit(uv[1], uv[0], 16), __builtin_amdgcn_alignbit(uv[2], uv[1], 16),
                                       __builtin_amdgcn_alignbit(uv[3], uv[2], 16), __builtin_amdgcn_alignbit(unx, uv[3], 16)});
      if (k == 19) {
        const u32 za = g < 4 ? LDS_U + ch * U_CH + ((g + 3) & 1) * 16 + (((g + 3) >> 1) & 1) * U_COPY : LDS_KX + ch * KX_STRIDE + (g & 31) * 16;
        lds_st<u32x4>(za, u32x4{0, 0, 0, g == 0 ? uv[0] << 16 : 0u});
      }
      if (k == 24) {
#pragma unroll
        for (int j = 0; j < 8; ++j) PIN(kg[j]);
        lds_st<u32x4>(LDS_KX + ch * KX_STRIDE + 512 + g * 16, u32x4{kg[0] | (kg[1] << 16), kg[2] | (kg[3] << 16), kg[4] | (kg[5] << 16), kg[6] | (g == 0 ? 0u : kg[7] << 16)});
      }
    });
    epi(3, [&](int k) { w2_rd(20 + k); });
  }
  STAMP(38);
  BAR();
  STAMP(6);
  rsrc_t rh = mk_rsrc(P.ws + WS_H, H_BYTES);
  const u32 hso = (u32)(((tt * 4 + th * 2) * 56 + kq * 14) * 1024);
  u32x4 hf[28];
  auto load_h = [&](int i) { hf[i] = ld16_async(rh, lane * 16, hso + ((i & 1) * 56 + (i >> 1)) * 1024); };
  const int trow = tt * 64 + th * 32 + lc, dcol = nb2 * 64 + 16 * kq + 4 * lg;
  u32x2 xres[2];
  u32x2 uu[2];
  {
    f32x4 ya[8];
#pragma unroll
    for (int T = 0; T < 8; ++T) ya[T] = f32x4{0.f, 0.f, 0.f, 0.f};
    const int s00 = 16 * (lg >> 1) + lc - 8 * (lg & 1) - 7;
    const int par = s00 & 1;
    const u32 abase = LDS_U + ch * U_CH + par * U_COPY + 32 + (s00 - par) * 2 + v4 * 128;
    const u32 bbase = LDS_KX + ch * KX_STRIDE + 512 + (16 * (lc - (lg >> 1)) + 8 * (lg & 1)) * 2 - v4 * 128 - 64;
    auto rdA = [&](int ph, int mu) {
      const u32 a = abase + ph * 64 + mu * 512;
      return u32x4{lds_ld<u32>(a), lds_ld<u32>(a + 4), lds_ld<u32>(a + 8), lds_ld<u32>(a + 12)};
    };
    const char* pfh = P.ws + WS_FH + (tt * 64 + 14 * kq) * FS;
    u64 fh = 0;
    u32x4 B[8], A[3];
    A[0] = rdA(0, 0);
#pragma unroll
    for (int g = 0; g < 8; ++g) B[g] = lds_ld<u32x4>(bbase + 64 + g * 512);
    A[1] = rdA(0, 1);
#pragma unroll
    for (int st = 0; st < 16; ++st) {
      const int ph = st >> 3, mu = st & 7;
      SCHED();
      if (st + 2 < 16) A[(st + 2) % 3] = rdA((st + 2) >> 3, (st + 2) & 7);
      SCHED();
#pragma unroll
      for (int T = 7; T >= mu; --T) {
        ya[T] = mma_h(A[st % 3], B[T - mu], ya[T]);
        if (T == 7 && ph == 0) { SCHED(); B[7 - mu] = lds_ld<u32x4>(bbase + (7 - mu) * 512); SCHED(); }
      }
      SCHED();
      if (st == PUBMU) {
        STAMP(39);
        WAIT_V(0);
        STAMP(40);
        if (!is_aux && lane == 0) st_agent(P.ws + WS_FH + ((mb * 2 + th) * 64 + nb * 4 + kq) * FS, tag);
      }
      if (st > PUBMU && st <= PUBMU + 6) {
        const int q = st - PUBMU - 1;
        w2_piece(rw2, vw2, sw2, 6 + 3 * (q / 3), 2 * (q % 3), (q / 3) * SLOT, lw2);
        w2_piece(rw2, vw2, sw2, 6 + 3 * (q / 3), 2 * (q % 3) + 1, (q / 3) * SLOT, lw2);
      }
      if (st == 11) {
#pragma unroll
        for (int t = 0; t < 2; ++t)
          asm volatile("buffer_load_dwordx2 %0, %1, %2, %3 offen" : "=v"(xres[t]) : "v"((u32)((trow * D + dcol) * 2)), "s"(rx), "s"(t * 16 * D * 2) : "memory");
#pragma unroll
        for (int i = 0; i < 2; ++i) uu[i] = lds_ld<u32x2>(LDS_U + ch * U_CH + 32 + (256 * (2 * v4 + i) + 16 * lc + 4 * lg) * 2);
      }
      if (st == 13) { BAR(); fh = ld8_async(pfh + (lane < 14 ? lane : 0) * FS); }
      if (st >= 13) {
#pragma unroll
        for (int T = (st == 13 ? 0 : mu - 1); T < mu; ++T) lds_st<f32x4>(SCR + (((ch * 4 + (T >> 1)) * 4 + v4) * 2 + (T & 1)) * 1024 + lane * 16, ya[T]);
      }
    }
    STAMP(7);
    lds_st<f32x4>(SCR + (((ch * 4 + 3) * 4 + v4) * 2 + 1) * 1024 + lane * 16, ya[7]);
    STAMP(41);
    WAIT_V(0);
    PIN(fh);
    if (!__all(fh == tag)) wait_flags(pfh, 14, tag, lane);
    STAMP(8);
#pragma unroll
    for (int i = 0; i < HW; ++i) load_h(i);
    BAR();
  }
  STAMP(9); CSTAMP(61);
  {
    f32x4 oa[4][2];
#pragma unroll
    for (int i = 0; i < 8; ++i) oa[i >> 1][i & 1] = f32x4{0.f, 0.f, 0.f, 0.f};
    rsrc_t ra = mk_rsrc(P.ws + WS_AO, AO_BYTES);
    const char* pfa = P.ws + WS_FA + ((tt >> 3) * 512 + nb2 * 64 + 16 * kq) * FS;
    u64 fa = 0;
    u32 ao[8];
    u32x4 cw[4];
    u32x2 nw[8];
    f32x4 y[4];
#pragma unroll
    for (int s = 0; s < 14; ++s) {
      if (HW + 2 * s < 28) { load_h(HW + 2 * s); load_h(HW + 2 * s + 1); }
      if (s < 12) asm volatile("s_waitcnt vmcnt(%2)" : "+v"(hf[2 * s]), "+v"(hf[2 * s + 1]) : "n"((int)((G2W >> (5 * s)) & 31)) : "memory");
      else { PIN(hf[2 * s]); PIN(hf[2 * s + 1]); }
      if (s == 0) STAMP(46);
      if (s == 3) BAR();
      if (s == 11) { STAMP(50); WAIT_V(1); BAR(); STAMP(51); }
#pragma unroll
      for (int i = 0; i < 8; ++i) {
        SCHED();
        oa[i >> 1][i & 1] = mma_bf(s < 6 ? wr[s < 6 ? s : 0][i >> 1] : cw[i >> 1], hf[2 * s + (i & 1)], oa[i >> 1][i & 1]);
        SCHED();
        if ((s == 1 || s == 2) && i == 1) {
          const int q = s - 1;
          const f32x4 ys = (y[0] + y[1]) + (y[2] + y[3]);
          float o[4];
#pragma unroll
          for (int r = 0; r < 4; ++r) {
            float u = h2f((uu[q][r >> 1] >> (16 * (r & 1))) & 0xffff);
            o[r] = fmaf(ys[r], kv.inv, u * kv.dsk);
          }
          u32x2 ov = {pk_bf16(o[0], o[1]), pk_bf16(o[2], o[3])};
          __builtin_amdgcn_raw_buffer_store_b64(ov, ra, (2 * b + ch) * AO_ROW + (256 * (2 * v4 + q) + 16 * lc + 4 * lg) * 2, 0, 16);
        }
        if (s < 2 && i >= 4) y[i - 4] = lds_ld<f32x4>(SCR + (((ch * 4 + v4) * 4 + i - 4) * 2 + s) * 1024 + lane * 16);
        if (s >= 3 && s <= 6 && i == 3) w2_piece(rw2, vw2, sw2, 12, s - 3, 2 * SLOT, lw2);
        if (s == 6 && i == 5) {
          WAIT_V(G2_AO);
          const char* p = P.ws + WS_FA + (v4 * 512 + 2 * b + ch) * FS;
          asm volatile("global_store_dwordx2 %0, %1, off sc1" :: "v"(p), "v"(tag) : "memory");
        }
        if (s == 10 && i == 0) fa = ld8_async(pfa + lc * FS);
        if (s == 12 && i == 7) {
          WAIT_V(0);
          PIN(fa);
          if (!__all(fa == tag)) wait_flags(pfa, 16, tag, lane);
#pragma unroll
          for (int e = 0; e < 8; ++e)
            asm volatile("buffer_load_ushort %0, %1, %2, %3 offen sc1" : "=v"(ao[e]) : "v"((u32)(dcol * AO_ROW + trow * 2)), "s"(ra), "s"((e & 3) * AO_ROW + (e >> 2) * 32) : "memory");
          STAMP(52);
        }
        if (s >= 5 && s < 13) {
          const int n = s + 1 - 6;
          nw[i] = lds_tr(w2a[i >> 1] + (n / 3) * SLOT + (n % 3) * 4096 + (i & 1) * 2048);
        }
      }
      SCHED();
#pragma unroll
      for (int g = 0; g < 4; ++g) cw[g] = cat(nw[2 * g], nw[2 * g + 1]);
    }
    STAMP(10);
#pragma unroll
    for (int g = 1; g < 4; ++g) {
      const u32 a = kq * 12288 + (th * 3 + g - 1) * 2048 + lane * 16;
      lds_st<f32x4>(a, oa[g][0]);
      lds_st<f32x4>(a + 1024, oa[g][1]);
    }
    WAIT_V(0);
    PIN(xres[0]); PIN(xres[1]);
#pragma unroll
    for (int e = 0; e < 8; ++e) {
      PIN(ao[e]);
      const u32 xw = xres[e >> 2][(e >> 1) & 1];
      oa[0][e >> 2][e & 3] += ((e & 1) ? bfhi(xw) : bf2f(xw)) + bf2f(ao[e]);
    }
    BAR();
#pragma unroll
    for (int g = 1; g < 4; ++g) {
      const u32 a = (kq ^ g) * 12288 + (th * 3 + g - 1) * 2048 + lane * 16;
      oa[0][0] += lds_ld<f32x4>(a); oa[0][1] += lds_ld<f32x4>(a + 1024);
    }
#pragma unroll
    for (int t = 0; t < 2; ++t)
      *(u32x2*)(P.out + (size_t)(trow + 16 * t) * D + dcol) = u32x2{pk_bf16(oa[0][t][0], oa[0][t][1]), pk_bf16(oa[0][t][2], oa[0][t][3])};
    STAMP(11); CSTAMP(62);
    STAMP_OUT;
    if (b == 255 && w == 0) {
#pragma unroll 1
      for (u32 sp = 0; sp < SPIN_CAP; ++sp) {
        u64 v0 = ld_agent(P.ws + WS_FA + lane * 2 * FS), v1 = ld_agent(P.ws + WS_FA + (128 + lane * 2) * FS);
        u64 v2 = ld_agent(P.ws + WS_FA + (256 + lane * 2) * FS), v3 = ld_agent(P.ws + WS_FA + (384 + lane * 2) * FS);
        if (__all(v0 == tag && v1 == tag && v2 == tag && v3 == tag)) break;
        __builtin_amdgcn_s_sleep(1);
      }
      if (tid == 0) st_agent(P.ws + WS_EP, tag);
    }
  }
}

extern "C" void kernel_launch(void* const* d_in, const int* in_sizes, int n_in,
                              void* d_out, int out_size, void* d_ws, size_t ws_size,
                              hipStream_t stream) {
  Params P;
  P.x = (const u16*)d_in[0]; P.lar = (const u16*)d_in[1]; P.aim = (const u16*)d_in[2];
  P.cre = (const u16*)d_in[3]; P.cim = (const u16*)d_in[4]; P.ldt = (const u16*)d_in[5];
  P.dsk = (const u16*)d_in[6]; P.w1 = (const u16*)d_in[8]; P.w3 = (const u16*)d_in[9];
  P.w2 = (const u16*)d_in[10]; P.out = (u16*)d_out; P.ws = (char*)d_ws;
  (void)hipFuncSetAttribute((const void*)fused, hipFuncAttributeMaxDynamicSharedMemorySize, LDS_TOTAL);
  fused<<<256, 512, LDS_TOTAL, stream>>>(P);
}
```

```cpp
#define PF 1
#include <hip/hip_runtime.h>
#include <stdint.h>
#if defined(__HIP_DEVICE_COMPILE__)
#pragma clang attribute push (__attribute__((target("no-packed-fp32-ops"))), apply_to = function)
#endif

typedef unsigned short u16;
typedef unsigned int u32;
typedef unsigned long long u64;
typedef __attribute__((ext_vector_type(4))) short s16x4;
typedef __attribute__((ext_vector_type(8))) __bf16 bf16x8;
typedef __attribute__((ext_vector_type(8))) _Float16 f16x8;
typedef __attribute__((ext_vector_type(2))) __bf16 bf16x2;
typedef __attribute__((ext_vector_type(2))) _Float16 f16x2;
typedef __attribute__((ext_vector_type(16))) float f32x16;
typedef __attribute__((ext_vector_type(4))) float f32x4;
typedef __attribute__((ext_vector_type(2))) float f32x2;
typedef __attribute__((ext_vector_type(4))) u32 u32x4;
typedef __attribute__((ext_vector_type(2))) u32 u32x2;
#define LAS __attribute__((address_space(3)))
#define GAS __attribute__((address_space(1)))
#define DEV __device__ __forceinline__
typedef __amdgpu_buffer_rsrc_t rsrc_t;

static constexpr int L = 2048, D = 512, NS = 128, F = 1792;

static constexpr size_t WS_H = 0;
static constexpr size_t WS_XT = 8u << 20;
static constexpr size_t WS_AO = 11u << 20;
static constexpr size_t WS_ST = 16u << 20;
static constexpr size_t WS_CS = WS_ST + (64u << 10);
static constexpr int FS = 128;
static constexpr size_t WS_FX = WS_ST + (128u << 10);
static constexpr size_t WS_FH = WS_FX + 32 * FS;
static constexpr size_t WS_EP = WS_FH + 2048 * FS;
static constexpr int H_BYTES = L * F * 2;
static constexpr int XT_BLK = 65536 + 256, XT_BYTES = 32 * XT_BLK;
static constexpr int AO_ROW = 16384 + 128, AO_BYTES = 256 * AO_ROW;
static constexpr u32 OOB = 0x7fffffffu;

static constexpr int SLOT = 49152;
static constexpr int LDS_TOTAL = 163840;
static constexpr int SCR = 2 * SLOT;
static constexpr int LDS_SC = LDS_TOTAL - 64;
static constexpr int KG_ROW = 112, KG_PLANE = 128 * KG_ROW;
static constexpr int LDS_KD = SCR + 4 * KG_PLANE;
static constexpr int U_COPY = 4160;
static constexpr int U_CH = 2 * U_COPY;
static constexpr int LDS_U = SCR;
static constexpr int KX_STRIDE = 4608;
static constexpr int LDS_KX = SCR + 2 * U_CH;

struct Params {
  const u16 *x, *lar, *aim, *cre, *cim, *ldt, *dsk, *w1, *w3, *w2;
  u16* out;
  char* ws;
};

DEV float asf(u32 v) { return __builtin_bit_cast(float, v); }
DEV u32 asu(float v) { return __builtin_bit_cast(u32, v); }
DEV float bf2f(u32 v) { return asf(v << 16); }
DEV float bfhi(u32 v) { return asf(v & 0xffff0000u); }
DEV u32 pk_bf16(float a, float b) { f32x2 p = {a, b}; return __builtin_bit_cast(u32, __builtin_convertvector(p, bf16x2)); }
DEV u32 pk_f16(float a, float b) { f32x2 p = {a, b}; return __builtin_bit_cast(u32, __builtin_convertvector(p, f16x2)); }
DEV float rbf(float a) { return bf2f(pk_bf16(a, a)); }
DEV float h2f(u32 v) { return (float)__builtin_bit_cast(_Float16, (u16)v); }
DEV bool all_of(bool p) { return __builtin_amdgcn_ballot_w64(!p) == 0; }
#define FMA __builtin_fmaf
#define AI __attribute__((always_inline))
template <int I> struct IC { constexpr operator int() const { return I; } };
template <int B, int E, typename FN> DEV void sfor(FN&& f) { if constexpr (B < E) { f(IC<B>{}); sfor<B + 1, E>(f); } }

template <typename T> DEV T lds_ld(u32 a) { return *(const LAS T*)a; }
template <typename T> DEV void lds_st(u32 a, T v) { *(LAS T*)a = v; }
DEV u32x2 lds_tr(u32 a) { return __builtin_bit_cast(u32x2, __builtin_amdgcn_ds_read_tr16_b64_v4i16((LAS s16x4*)a)); }
DEV u32x4 cat(u32x2 a, u32x2 b) { return __builtin_shufflevector(a, b, 0, 1, 2, 3); }
DEV f32x16 mma32(u32x4 a, u32x4 b, f32x16 c) { return __builtin_amdgcn_mfma_f32_32x32x16_bf16(__builtin_bit_cast(bf16x8, a), __builtin_bit_cast(bf16x8, b), c, 0, 0, 0); }
DEV f32x4 mma_bf(u32x4 a, u32x4 b, f32x4 c) { return __builtin_amdgcn_mfma_f32_16x16x32_bf16(__builtin_bit_cast(bf16x8, a), __builtin_bit_cast(bf16x8, b), c, 0, 0, 0); }
DEV f32x4 mma_h(u32x4 a, u32x4 b, f32x4 c) { return __builtin_amdgcn_mfma_f32_16x16x32_f16(__builtin_bit_cast(f16x8, a), __builtin_bit_cast(f16x8, b), c, 0, 0, 0); }
DEV rsrc_t mk_rsrc(const void* p, u32 bytes) { return __builtin_amdgcn_make_buffer_rsrc((void*)p, 0, bytes, 0x00020000); }
DEV void dma16(rsrc_t rs, u32 voff, u32 sbase, int simm, u32 lbase, int limm) {
  u32 t;
  asm volatile("s_add_u32 m0, %1, %2\n\ts_add_u32 %0, %3, %4\n\tbuffer_load_dwordx4 %5, %6, %0 offen lds"
               : "=&s"(t) : "s"(lbase), "n"(limm), "s"(sbase), "n"(simm), "v"(voff), "s"(rs) : "memory", "scc");
}
#define PINS(x) asm volatile("" : "+s"(x))
#define WAIT_V(n) asm volatile("s_waitcnt vmcnt(%0)" :: "n"(n) : "memory")
#define BAR() asm volatile("s_waitcnt lgkmcnt(0)\n\ts_barrier" ::: "memory")
#define SCHED() __builtin_amdgcn_sched_barrier(0)
#define PIN(x) asm volatile("" : "+v"(x))
#define PIN2(x, y) asm volatile("" : "+v"(x), "+v"(y))
#define RLX __ATOMIC_RELAXED, __HIP_MEMORY_SCOPE_AGENT
DEV u64 ld_agent(const char* p) { return __hip_atomic_load((GAS u64*)p, RLX); }
DEV void st_agent(char* p, u64 v) { __hip_atomic_store((GAS u64*)p, v, RLX); }
static constexpr u32 CODE_BYTES = 21 * 1024;
static constexpr u32 SPIN_CAP = 1u << 16;

#define STAMP(i) do {} while (0)
#define STAMP_DECL
#define CSTAMP(i)
#define STAMP_OUT

DEV void wait_flags(const char* p, u64 tag) {
#pragma unroll 1
  for (u32 s = 0; s < SPIN_CAP; ++s) {
    u64 v = ld_agent(p);
    if (all_of(v == tag)) break;
    __builtin_amdgcn_s_sleep(1);
  }
  asm volatile("" ::: "memory");
}
DEV u64 ld8_async(const char* p) {
  u64 v;
  asm volatile("global_load_dwordx2 %0, %1, off sc1" : "=v"(v) : "v"(p) : "memory");
  return v;
}
DEV u32x4 ld16_async(rsrc_t rs, u32 voff, u32 soff) {
  u32x4 v;
  asm volatile("buffer_load_dwordx4 %0, %1, %2, %3 offen sc1" : "=v"(v) : "v"(voff), "s"(rs), "s"(soff) : "memory");
  return v;
}
DEV u32 ld_u16_async(const u16* base, u32 off) {
  u32 v;
  asm volatile("global_load_ushort %0, %1, %2" : "=v"(v) : "v"(off), "s"(base) : "memory");
  return v;
}
template <int C> DEV float dppf(float v) { return asf((u32)__builtin_amdgcn_update_dpp(0, (int)asu(v), C, 0xf, 0xf, true)); }
DEV float row_sum(float v) { v += dppf<0x128>(v); v += dppf<0x124>(v); v += dppf<0x122>(v); v += dppf<0x121>(v); return v; }
DEV float row_max(float v) { v = __builtin_fmaxf(v, dppf<0x128>(v)); v = __builtin_fmaxf(v, dppf<0x124>(v)); v = __builtin_fmaxf(v, dppf<0x122>(v)); v = __builtin_fmaxf(v, dppf<0x121>(v)); return v; }
DEV float fold32(float a, float b) {
  auto r = __builtin_amdgcn_permlane32_swap(asu(a), asu(b), false, false);
  return asf(r[0]) + asf(r[1]);
}
DEV float fold16(float a, float b) {
  auto r = __builtin_amdgcn_permlane16_swap(asu(a), asu(b), false, false);
  return asf(r[0]) + asf(r[1]);
}
DEV float dot2bf(u32 a, u32 b, float c) { return __builtin_amdgcn_fdot2_f32_bf16(__builtin_bit_cast(bf16x2, a), __builtin_bit_cast(bf16x2, b), c, false); }
DEV u32 rdu(float v, int l) { return (u32)__builtin_amdgcn_readlane((int)asu(v), l); }
DEV float rdl(float v, int l) { return asf(rdu(v, l)); }
DEV u32 umax(u32 a, u32 b) { return a > b ? a : b; }
DEV u64 mk_tag(u64 ep) {
  u32 lo, hi;
  asm volatile("v_readfirstlane_b32 %0, %2\n\tv_readfirstlane_b32 %1, %3" : "=s"(lo), "=s"(hi) : "v"((u32)ep), "v"((u32)(ep >> 32)) : "memory");
  return (((u64)hi << 32) | lo) * 6364136223846793005ull + 1442695040888963407ull;
}

struct cpx { float r, i; };
DEV cpx cmul(cpx a, cpx b) { return cpx{FMA(a.r, b.r, -(a.i * b.i)), FMA(a.r, b.i, a.i * b.r)}; }
DEV cpx csqr(cpx a) { return cpx{FMA(a.r, a.r, -(a.i * a.i)), (a.r + a.r) * a.i}; }
DEV cpx cexp_at(float ar2, float airev, float p) {
  float e = __builtin_amdgcn_exp2f(ar2 * p);
  float fr = __builtin_amdgcn_fractf(airev * p);
  return cpx{e * __builtin_amdgcn_cosf(fr), e * __builtin_amdgcn_sinf(fr)};
}
struct KgIn { u32 lar, aim, cr, ci, cr2, ci2, ldt, dsk; };
struct KgSeed { cpx v[32], m[3], qa[2], qb[2]; float inv, dsk; };
DEV KgIn kgen_load(const Params& P, int c, int w, int lane) {
  const int d = 2 * c + (w >> 2);
  const u32 o = (d * NS + 64 * ((w >> 1) & 1) + lane) * 2, o2 = o ^ 128, od = d * 2;
  KgIn k;
  k.lar = ld_u16_async(P.lar, o); k.aim = ld_u16_async(P.aim, o);
  k.cr = ld_u16_async(P.cre, o); k.ci = ld_u16_async(P.cim, o);
  k.cr2 = ld_u16_async(P.cre, o2); k.ci2 = ld_u16_async(P.cim, o2);
  k.ldt = ld_u16_async(P.ldt, od); k.dsk = ld_u16_async(P.dsk, od);
  return k;
}
template <typename FN> DEV void kgen_seed(KgSeed& o, KgIn k, int w, FN&& side) {
  asm volatile("" : "+v"(k.lar), "+v"(k.aim), "+v"(k.cr), "+v"(k.ci), "+v"(k.cr2), "+v"(k.ci2), "+v"(k.ldt), "+v"(k.dsk));
  const float cr = bf2f(k.cr), ci = bf2f(k.ci), cr2 = bf2f(k.cr2);
  const float dt = rbf(__builtin_amdgcn_exp2f(bf2f(k.ldt) * 1.44269504088896f));
  const float ar = -rbf(__builtin_amdgcn_exp2f(bf2f(k.lar) * 1.44269504088896f)) * dt, ai = bf2f(k.aim) * dt;
  const float ar2 = ar * 1.44269504088896f, airev = ai * 0.159154943091895f;
  side(0);
  const float mx = row_max(__builtin_fmaxf(__builtin_fabsf(cr) + __builtin_fabsf(ci), __builtin_fabsf(cr2) + __builtin_fabsf(bf2f(k.ci2))));
  const float sm = row_sum(cr + cr2);
  side(1);
  o.dsk = bf2f(k.dsk) + 2.f * dt * ((rdl(sm, 0) + rdl(sm, 16)) + (rdl(sm, 32) + rdl(sm, 48)));
  const float M = 2.f * dt * asf(umax(umax(rdu(mx, 0), rdu(mx, 16)), umax(rdu(mx, 32), rdu(mx, 48))));
  int sexp = 261 - (int)((asu(M) >> 23) & 0xff);
  sexp = sexp > 254 ? 254 : sexp;
  o.inv = asf((u32)(254 - sexp) << 23);
  const float cs = 2.f * dt * asf((u32)sexp << 23);
  const cpx C{cr * cs, ci * cs};
  side(2);
  const bool odd = w & 1;
  const cpx w128 = cexp_at(ar2, airev, 128.f), w256 = csqr(w128), w512 = csqr(w256);
  side(3);
  const cpx s1 = cexp_at(ar2, odd ? airev : -airev, odd ? 1.f : 8.f), s2 = csqr(s1), s4 = csqr(s2), s8 = csqr(s4);
  side(4);
  o.m[0] = s2; o.m[1] = s4;
  if (odd) {
    o.v[0] = cmul(C, s1); o.v[1] = cmul(C, s2); o.m[2] = w128;
    const cpx c4 = cmul(C, s4);
    o.v[16] = cmul(o.v[1], w256); o.v[17] = cmul(c4, w256); o.qa[0] = s4; o.qb[0] = w128;
    o.v[24] = cmul(c4, w512); o.v[25] = cmul(o.v[24], s4); o.qa[1] = w128; o.qb[1] = w256;
  } else {
    o.v[0] = cpx{1.f, 0.f}; o.v[1] = s1; o.m[2] = s8;
    o.v[16] = cmul(cmul(C, csqr(w512)), cpx{s1.r, -s1.i}); o.v[17] = cmul(o.v[16], w128); o.qa[0] = w256; o.qb[0] = w512;
    o.v[24] = o.v[16]; o.v[25] = o.v[17]; o.qa[1] = w256; o.qb[1] = w512;
  }
  side(5);
}
struct KdTab { u16 v[2048]; };
constexpr KdTab make_kd() {
  KdTab t{};
  for (int i = 0; i < 2048; ++i) {
    int l = (i & ~7) + 7 - (i & 7);
    if (l == 0) l = 1;
    int sh = l < 256 ? 0 : l < 512 ? 1 : l < 1024 ? 2 : 3;
    int q = l >> sh, rem = l - (q << sh), half = (1 << sh) >> 1;
    if (sh > 0 && (rem > half || (rem == half && (q & 1)))) ++q;
    int p = (q << sh) - 1;
    int I = p >> 7, sg = I < 2 ? 0 : I < 4 ? 1 : I < 8 ? 2 : 3;
    int r = 8 * sg + (I << (3 - sg)) + ((p & 7) >> sg);
    t.v[i] = (u16)((r * 16 + ((p >> 3) & 15)) * 2);
  }
  return t;
}
__constant__ __attribute__((aligned(16))) KdTab kd_tab = make_kd();

#define HW 14
static constexpr int PD = 4;
constexpr int g2_ops_at(int s) { return (HW + s < 28 ? 1 : 0) + (s == 3 ? 2 : 0) + (s >= 5 && s <= 8 ? 1 : 0) + (s >= 20 && s <= 23 ? 1 : 0); }
constexpr int g2_issued_through(int s) { int n = HW; for (int i = 0; i <= s; ++i) n += g2_ops_at(i); return n; }
constexpr int g2_pos_hf(int i) { return i < HW ? i + 1 : g2_issued_through(i - HW - 1) + 1; }
constexpr int g2_wait_hf(int s) { return g2_issued_through(s - 1) + (HW + s < 28 ? 1 : 0) - g2_pos_hf(s); }
static constexpr int G2_E = g2_issued_through(23 - PD) + (HW + 24 - PD < 28 ? 1 : 0) - g2_issued_through(8);

__global__ __launch_bounds__(512, 2) void fused(Params P) {
  const int b = __builtin_amdgcn_workgroup_id_x(), xcd = b & 7, idx = b >> 3;
  const int tid = __builtin_amdgcn_workitem_id_x(), lane = tid & 63;
  const int w = __builtin_amdgcn_readfirstlane(tid >> 6);
  STAMP_DECL;
  STAMP(0); CSTAMP(58);
  KgIn kin = kgen_load(P, b, w, lane);
  u64 ep;
  asm volatile("global_load_dwordx2 %0, %1, %2 sc1" : "=v"(ep) : "v"((u32)WS_EP), "s"(P.ws) : "memory");
  const bool is_aux = idx >= 28;
  {
    u64 pc;
    asm volatile("s_getpc_b64 %0" : "=s"(pc));
    rsrc_t rcode = mk_rsrc((const void*)(pc & ~(u64)255), CODE_BYTES);
    dma16(rcode, w == 7 ? idx * 1024 + lane * 16 : OOB, 0, 0, 3 * SLOT, 0);
  }
  const int mb = (xcd & 3) * 4 + (idx & 3), nb = (xcd >> 2) * 7 + (idx >> 2);
  const int tt = xcd * 4 + (idx & 3), nb2 = idx >> 2;
  const int th = w & 1, fq = w >> 1;
  const int ch = w >> 2, v4 = w & 3;
  const int lc = lane & 15, lg = lane >> 4, lq = (lane & 15) >> 2, lp = lane & 3, lh = lane >> 5;
  rsrc_t rx = mk_rsrc(P.x, L * D * 2);
  rsrc_t rw = mk_rsrc(w >= 4 ? P.w3 : P.w1, D * F * 2);
  const u32 vx0 = (lane >> 3) * 1024 + (((lane & 7) ^ lg) * 16);
  const u32 vx1 = vx0 ^ 64;
  const u32 vw = lg * (F * 2) + ((lc ^ (lg << 2)) * 16);
  const u32 sx0 = (u32)((mb * 128 + 16 * w) * 1024), sw0 = (u32)(16 * (w & 3) * (F * 2) + nb * 256);
  const u32 lx0 = w * 2048, lw0 = w * 4096;
  auto g1_piece = [&](int s, int i) AI {
    const int slot = ((s + 1) % 3) * SLOT;
    if (i == 0) dma16(rx, vx0, sx0, s * 128, lx0, slot);
    else if (i == 1) dma16(rx, vx1, sx0, s * 128 + 8192, lx0, slot + 1024);
    else dma16(rw, vw, sw0, (s * 64 + (i - 2) * 4) * F * 2, lw0, slot + 16384 + (i - 2) * 1024);
  };
  u64 tag; u32 t32;
  KgSeed kg;
  rsrc_t rw2 = mk_rsrc(P.w2, F * D * 2);
  const u32 vw2 = (lane >> 3) * 1024 + (((lane & 7) ^ ((lg & 1) << 2)) * 16);
  u32 sw2 = (u32)((v4 * 448 + ch * 8) * 1024 + nb2 * 128), lw2 = v4 * 12288 + ch * 1024;
  PINS(sw2); PINS(lw2);
  auto w2_piece = [&](int k, int slot) AI { dma16(rw2, vw2, sw2, k * 16384, lw2, slot + (k % 6) * 2048); };
  rsrc_t rxt = mk_rsrc(P.ws + WS_XT, XT_BYTES);

  u32 kre[16], kim[16];
  auto kx_mul = [&](int d, int s, cpx m) AI { kg.v[d] = cmul(kg.v[s], m); PIN2(kg.v[d].r, kg.v[d].i); };
  auto kx_pack = [&](int p) AI { kre[p] = pk_f16(kg.v[2 * p].r, kg.v[2 * p + 1].r); kim[p] = pk_f16(kg.v[2 * p].i, kg.v[2 * p + 1].i); PIN2(kre[p], kim[p]); };
  auto kx_piece = [&](int k) AI {
    if (k < 20) {
      const int q = k / 10, j = k % 10, o = 16 + 8 * q;
      if (j < 2) kx_mul(o + 2 + j, o + j, kg.qa[q]);
      else if (j < 6) kx_mul(o + 2 + j, o + j - 2, kg.qb[q]);
      else kx_pack(o / 2 + j - 6);
    } else {
      const int j = k - 20;
      if (j < 2) kx_mul(2 + j, j, kg.m[0]);
      else if (j < 6) kx_mul(2 + j, j - 2, kg.m[1]);
      else {
        const int p = (j - 6) >> 2, r = (j - 6) & 3;
        if (r < 2) kx_mul(8 + 2 * p + r, 2 * p + r, kg.m[2]);
        else kx_pack(p + 4 * (r - 2));
      }
    }
  };
  auto kt_store = [&](int i) AI {
    const int c = i >> 1;
    const u32 kbase = SCR + ch * 2 * KG_PLANE + (64 * ((w >> 1) & 1) + lane) * KG_ROW + (th ? c * 16 : (c < 2 ? 80 + c * 16 : 64));
    const u32* p = (i & 1) ? kim : kre;
    lds_st<u32x4>(kbase + (i & 1) * KG_PLANE, u32x4{p[4 * c], p[4 * c + 1], p[4 * c + 2], p[4 * c + 3]});
  };
  const int kt = v4 < 3 ? v4 : 0;
  const u32 krb = SCR + ch * 2 * KG_PLANE + (8 * lg + lq) * KG_ROW + lp * 8, kra = krb + kt * 32;
  u32x4 kA[8], kB[8];
  f32x4 ka = {0.f, 0.f, 0.f, 0.f}, kb = ka;
  auto kt_rd = [&](int k) AI {
    const u32 o = k * 32 * KG_ROW;
    kA[k] = cat(lds_tr(kra + o), lds_tr(kra + o + 4 * KG_ROW)); kB[k] = cat(lds_tr(krb + 80 + o), lds_tr(krb + 80 + o + 4 * KG_ROW));
  };
  auto kt_mma = [&](int k) AI { if (k & 1) kb = mma_h(kB[k], kA[k], kb); else ka = mma_h(kB[k], kA[k], ka); };
  auto kt_put = [&]() AI {
    ka += kb;
    lds_st<f32x2>(LDS_SC + ch * 8, f32x2{kg.inv, kg.dsk});
    lds_st<u32x2>(LDS_KD + ch * 1536 + ((16 * kt + lc) * 16 + 4 * lg) * 2, u32x2{pk_f16(ka[0], ka[1]), pk_f16(ka[2], ka[3])});
  };
  u32x4 uv, kdv; u32 unx, kgv[8];
  const int g8 = tid & 255;
  auto load_u = [&]() AI {
    const u32 cb = (2 * b + ch) * 128, gn = g8 + 1;
    uv = ld16_async(rxt, (g8 >> 3) * XT_BLK + (g8 & 7) * 16 + cb, 0);
    asm volatile("buffer_load_dword %0, %1, %2, 0 offen sc1" : "=v"(unx) : "v"((gn >> 3) * XT_BLK + (gn & 7) * 16 + cb), "s"(rxt) : "memory");
    asm volatile("global_load_dwordx4 %0, %1, %2" : "=v"(kdv) : "v"(g8 * 16), "s"(&kd_tab) : "memory");
  };
  auto kt_fin = [&](int k) AI {
    if (k == 0) { PIN(uv); PIN(unx); PIN(kdv); }
    if (k < 4) { kgv[2 * k] = lds_ld<u16>(LDS_KD + ch * 1536 + (kdv[k] & 0xffffu)); kgv[2 * k + 1] = lds_ld<u16>(LDS_KD + ch * 1536 + (kdv[k] >> 16)); }
    if (k == 4) lds_st<u32x4>(LDS_U + ch * U_CH + 32 + g8 * 16, uv);
    if (k == 5) lds_st<u32x4>(LDS_U + ch * U_CH + U_COPY + 32 + g8 * 16,
                              u32x4{__builtin_amdgcn_alignbit(uv[1], uv[0], 16), __builtin_amdgcn_alignbit(uv[2], uv[1], 16),
                                    __builtin_amdgcn_alignbit(uv[3], uv[2], 16), __builtin_amdgcn_alignbit(unx, uv[3], 16)});
    if (k == 6) {
      u32 za = LDS_U + ch * U_CH + ((g8 + 3) & 1) * 16 + (((g8 + 3) >> 1) & 1) * U_COPY, zb = LDS_KX + ch * KX_STRIDE + (g8 & 31) * 16;
      PIN2(za, zb);
      lds_st<u32x4>(g8 < 4 ? za : zb, u32x4{0, 0, 0, g8 == 0 ? uv[0] << 16 : 0u});
    }
    if (k == 7) {
#pragma unroll
      for (int j = 0; j < 8; ++j) PIN(kgv[j]);
      lds_st<u32x4>(LDS_KX + ch * KX_STRIDE + 512 + g8 * 16, u32x4{kgv[0] | (kgv[1] << 16), kgv[2] | (kgv[3] << 16), kgv[4] | (kgv[5] << 16), kgv[6] | (g8 == 0 ? 0u : kgv[7] << 16)});
    }
  };
  const char* pfx = P.ws + WS_FX + (4 * w + lp) * FS;

  f32x16 ag[2], au[2];
#pragma unroll
  for (int i = 0; i < 16; ++i) { ag[0][i] = 0.f; ag[1][i] = 0.f; au[0][i] = 0.f; au[1][i] = 0.f; }
  float eke[2] = {1.f, 1.f}, es2[2] = {1.f, 1.f};
  rsrc_t rhs = mk_rsrc(P.ws + WS_H, H_BYTES);
  const u32 ho = (u32)(((mb * 4 + th * 2) * 112 + nb * 8 + fq * 2) * 1024 + lane * 16);
  float ee[4], eg[4]; u32x4 eo;
  auto epi_piece = [&](int t2, int k) AI {
    const int q = k / 5, j = k % 5;
#pragma unroll
    for (int i = 0; i < 4; ++i) {
      if (j == 0) { ee[i] = ag[t2][4 * q + i] * eke[t2]; eg[i] = ag[t2][4 * q + i] * au[t2][4 * q + i]; }
      if (j == 1) ee[i] = __builtin_amdgcn_exp2f(ee[i]);
      if (j == 2) ee[i] = FMA(ee[i], es2[t2], es2[t2]);
      if (j == 3) ee[i] = __builtin_amdgcn_rcpf(ee[i]);
      if (j == 4) eg[i] *= ee[i];
    }
    if (j < 4) asm volatile("" : "+v"(ee[0]), "+v"(ee[1]), "+v"(ee[2]), "+v"(ee[3]), "+v"(eg[0]), "+v"(eg[1]), "+v"(eg[2]), "+v"(eg[3]));
    if (j == 4) {
      eo[2 * (q & 1)] = pk_bf16(eg[0], eg[1]); eo[2 * (q & 1) + 1] = pk_bf16(eg[2], eg[3]);
      if (q & 1) __builtin_amdgcn_raw_buffer_store_b128(eo, rhs, ho + (t2 * 112 + (q >> 1)) * 1024, 0, 16);
      else PIN2(eo[0], eo[1]);
    }
  };

  if (__builtin_expect(!is_aux, 1)) {
#pragma unroll
    for (int i = 0; i < 6; ++i) g1_piece(0, i);
    STAMP(13);
    WAIT_V(6);
    tag = mk_tag(ep); t32 = (u32)tag;
    STAMP(1);
    kgen_seed(kg, kin, w, [&](int i) AI { g1_piece(1, i); });
    STAMP(2); CSTAMP(59);
    u32 xa[4], wa;
    {
      const int row = 64 * th + (lane & 31);
#pragma unroll
      for (int ks = 0; ks < 4; ++ks) xa[ks] = SLOT + row * 128 + (((2 * ks + lh) ^ ((row >> 1) & 7)) * 16);
      wa = SLOT + 16384 + (8 * lh + lq) * 256 + ((fq ^ lq) * 64) + (4 * (lg & 1) + ((lp & 1) << 1) + (lp >> 1)) * 8;
    }
    const int G = (lane & 31) >> 2;
    const char* pcs = P.ws + WS_CS + (size_t)(lh * F + nb * 128 + fq * 32 + (4 * (G >> 2) + 2 * (G & 1) + ((G >> 1) & 1)) * 4 + lp) * 8;
    const char* pst = P.ws + WS_ST + (size_t)(mb * 128 + 64 * th + (lane & 31)) * 8;
    u64 gc, gs[2], fx;
    u32x4 xg, xu, xm[2];
    WAIT_V(6);
    BAR();
    STAMP(3);
    u32x4 fx0[2], fx1[2], fg[2], fu[2];
    auto rd0 = [&](int d, int ks) AI { fx0[d] = lds_ld<u32x4>(xa[ks]); fg[d] = cat(lds_tr(wa + ks * 4096), lds_tr(wa + ks * 4096 + 1024)); };
    auto rd1 = [&](int d, int ks) AI { fu[d] = cat(lds_tr(wa + 16384 + ks * 4096), lds_tr(wa + 16384 + ks * 4096 + 1024)); fx1[d] = lds_ld<u32x4>(xa[ks] + 4096); };
    rd0(0, 0); rd1(0, 0);
    g1_piece(2, 0); g1_piece(2, 1);
    auto filler = [&](int n) AI {
      if (n < 84 && !(n & 1)) kx_piece(n >> 1);
      if (n == 104) { fx = ld8_async(pfx); gc = ld8_async(pcs); }
      if (n == 105) { gs[0] = ld8_async(pst); gs[1] = ld8_async(pst + 256); }
      if (n == 124) load_u();
      if (n == 120) {
        WAIT_V(6);
        PIN(gc); PIN(gs[0]); PIN(gs[1]); PIN(fx);
        auto ok = [&]() AI { return all_of((((u32)(gc >> 32) ^ t32) | ((u32)(gs[0] >> 32) ^ t32) | ((u32)(gs[1] >> 32) ^ t32) | ((u32)fx ^ t32) | ((u32)(fx >> 32) ^ (u32)(tag >> 32))) == 0); };
        if (__builtin_expect(!ok(), 0)) {
#pragma unroll 1
          for (u32 sp = 0; sp < SPIN_CAP; ++sp) {
            gc = ld_agent(pcs); gs[0] = ld_agent(pst); gs[1] = ld_agent(pst + 256); fx = ld_agent(pfx);
            if (ok()) break;
            __builtin_amdgcn_s_sleep(1);
          }
        }
      }
      if (n == 121) {
        const float cv = asf((u32)gc);
        const float c0 = rbf(cv), r1 = cv - c0, c1 = rbf(r1);
        const u32 e0 = pk_bf16(c0, c1), e1 = pk_bf16(r1 - c1, 0.f);
        xg = u32x4{lh ? 0u : e0, lh ? 0u : e1, 0, 0}; xu = u32x4{lh ? e0 : 0u, lh ? e1 : 0u, 0, 0};
        PIN2(xg, xu);
      }
      if (n == 122 || n == 123) {
        const int t = n - 122;
        const u32 m = ((u32)gs[t] & 0xffffu) ^ 0x8000u;
        xm[t] = u32x4{m | (m << 16), m, 0, 0};
        const float sd = bfhi((u32)gs[t]);
        eke[t] = -1.44269504088896f * __builtin_amdgcn_rcpf(sd); es2[t] = sd * sd;
        PIN(xm[t]); PIN2(eke[t], es2[t]);
      }
      if (n >= 124) kt_store(n - 124);
    };
    sfor<0, 32>([&](auto I) AI {
      constexpr int s = I / 4, ks = I % 4;
      if (ks == 3) {
        STAMP(16 + 2 * s);
        if (s == 6) WAIT_V(10); else if (s < 7) WAIT_V(6);
        BAR();
        if (s < 7) {
          const int dl = (s + 2) % 3 == 0 ? -2 * SLOT : SLOT;
#pragma unroll
          for (int i = 0; i < 4; ++i) { xa[i] += dl; PIN(xa[i]); }
          wa += dl; PIN(wa);
        }
        STAMP(17 + 2 * s);
      }
      const int n = s * 16 + ks * 4, kn = (ks + 1) & 3;
      const int rs = ks == 3 ? s : s - 1, pi = ks == 3 ? 0 : 2 * ks + 2;
      auto piece = [&](int i) AI {
        if (i > 5) return;
        if (rs + 3 < 8) g1_piece(rs + 3, i);
        else if (rs == 5) w2_piece(i, 0);
        else if (rs == 6) w2_piece(6 + i, SLOT);
      };
      SCHED(); ag[0] = mma32(fg[0], fx0[0], ag[0]); SCHED();
      if (I < 31) rd0(1, kn);
      filler(n);
      SCHED(); au[0] = mma32(fu[0], fx0[0], au[0]); SCHED();
      if (I < 31) rd1(1, kn);
      filler(n + 1);
      SCHED(); ag[1] = mma32(fg[0], fx1[0], ag[1]); SCHED();
      filler(n + 2); piece(pi);
      SCHED(); au[1] = mma32(fu[0], fx1[0], au[1]); SCHED();
      filler(n + 3); piece(pi + 1);
      SCHED();
      fx0[0] = fx0[1]; fx1[0] = fx1[1]; fg[0] = fg[1]; fu[0] = fu[1];
    });
    ag[0] = mma32(xg, xm[0], ag[0]); SCHED(); kt_store(4); SCHED();
    au[0] = mma32(xu, xm[0], au[0]); SCHED(); kt_store(5); SCHED();
    ag[1] = mma32(xg, xm[1], ag[1]); SCHED(); kt_store(6); SCHED();
    au[1] = mma32(xu, xm[1], au[1]); SCHED(); kt_store(7); SCHED();
    STAMP(4);
    sfor<0, 40>([&](auto I) AI { epi_piece(I / 20, I % 20); });
    WAIT_V(4);
  } else {
    const int a = xcd * 4 + idx - 28, ca = a < 28 ? a : 27;
    const int cmat = ca / 14, cnb = ca % 14;
    rsrc_t rc = mk_rsrc(cmat == 1 ? P.w3 : P.w1, D * F * 2);
    const u32 vo = (tid >> 4) * (F * 2) + cnb * 256 + lc * 16;
    u32x4 wv[16], xv[8];
    float s1[8], s2[8];
#pragma unroll
    for (int i = 0; i < 8; ++i) xv[i] = __builtin_amdgcn_raw_buffer_load_b128(rx, lane * 16, (64 * a + 8 * w + i) * 1024, 0);
    WAIT_V(8);
    tag = mk_tag(ep); t32 = (u32)tag;
#pragma unroll
    for (int i = 0; i < 8; ++i) {
      s1[i] = 0.f; s2[i] = 0.f;
#pragma unroll
      for (int j = 0; j < 4; ++j) { s1[i] = dot2bf(xv[i][j], 0x3f803f80u, s1[i]); s2[i] = dot2bf(xv[i][j], xv[i][j], s2[i]); }
      wv[i] = __builtin_amdgcn_raw_buffer_load_b128(rc, vo, i * 32 * F * 2, 0);
      SCHED();
    }
    float rs[2], mr[2];
#pragma unroll
    for (int g = 0; g < 2; ++g) {
      const float S = row_sum(fold16(fold32(s1[4 * g], s1[4 * g + 2]), fold32(s1[4 * g + 1], s1[4 * g + 3])));
      const float Q = row_sum(fold16(fold32(s2[4 * g], s2[4 * g + 2]), fold32(s2[4 * g + 1], s2[4 * g + 3])));
      const float mf = S * (1.f / D);
      const float var = rbf(__builtin_fmaxf(Q * (1.f / D) - mf * mf, 0.f));
      const float m = rbf(mf);
      const float sd = rbf(__builtin_amdgcn_sqrtf(rbf(var + 1e-5f)));
      rs[g] = __builtin_amdgcn_rcpf(sd);
      mr[g] = -m * rs[g];
      __builtin_amdgcn_raw_buffer_store_b64(u32x2{asu(sd) | (asu(m) >> 16), t32}, mk_rsrc(P.ws + WS_ST, L * 8), lc == 0 ? (u32)(64 * a + 8 * w + 4 * g + lg) * 8 : OOB, 0, 16);
    }
    float rr[8], cc[8];
#pragma unroll
    for (int i = 0; i < 8; ++i) { rr[i] = rdl(rs[i >> 2], (i & 3) * 16); cc[i] = rdl(mr[i >> 2], (i & 3) * 16); }
#pragma unroll
    for (int e = 0; e < 8; ++e) {
      u32x4 o;
#pragma unroll
      for (int j = 0; j < 4; ++j) {
        const u32 v0 = xv[2 * j][e >> 1], v1 = xv[2 * j + 1][e >> 1];
        o[j] = pk_f16(FMA((e & 1) ? bfhi(v0) : bf2f(v0), rr[2 * j], cc[2 * j]), FMA((e & 1) ? bfhi(v1) : bf2f(v1), rr[2 * j + 1], cc[2 * j + 1]));
      }
      lds_st<u32x4>((u32)((8 * lane + e) * 128 + ((w ^ (lane & 7)) * 16)), o);
      wv[8 + e] = __builtin_amdgcn_raw_buffer_load_b128(rc, vo, (8 + e) * 32 * F * 2, 0);
      SCHED();
    }
    BAR();
#pragma unroll
    for (int j = 0; j < 8; ++j) {
      const int d = (tid >> 3) + 64 * j, sl = tid & 7;
      __builtin_amdgcn_raw_buffer_store_b128(lds_ld<u32x4>((u32)(d * 128 + ((sl ^ ((d >> 3) & 7)) * 16))), rxt, a * XT_BLK + d * 128 + sl * 16, 0, 16);
    }
    u32x4 sel;
    const u32 one = (lc & 1) ? 0x3f800000u : 0x3f80u;
    sel[0] = (lc >> 1) == 0 ? one : 0; sel[1] = (lc >> 1) == 1 ? one : 0;
    sel[2] = (lc >> 1) == 2 ? one : 0; sel[3] = (lc >> 1) == 3 ? one : 0;
    f32x4 c0 = {0.f, 0.f, 0.f, 0.f}, c1 = c0;
#pragma unroll
    for (int k = 0; k < 16; k += 2) { c0 = mma_bf(sel, wv[k], c0); c1 = mma_bf(sel, wv[k + 1], c1); }
    if (lane < 32) lds_st<f32x4>(65536 + w * 512 + lc * 32 + lg * 16, c0 + c1);
    WAIT_V(0);
    BAR();
    if (tid == 0) st_agent(P.ws + WS_FX + a * FS, tag);
    if (tid < 128) {
      float sum = 0.f;
#pragma unroll
      for (int j = 0; j < 8; ++j) sum += lds_ld<float>(65536 + j * 512 + tid * 4);
      st_agent(P.ws + WS_CS + (size_t)(cmat * F + cnb * 128 + tid) * 8, ((u64)t32 << 32) | asu(sum));
    }
    STAMP(2); CSTAMP(59);
    kgen_seed(kg, kin, w, [&](int i) AI {});
#pragma unroll
    for (int k = 0; k < 42; ++k) kx_piece(k);
#pragma unroll
    for (int i = 0; i < 8; ++i) kt_store(i);
    STAMP(17);
    wait_flags(pfx, tag);
    BAR();
    load_u();
#pragma unroll
    for (int i = 0; i < 12; ++i) w2_piece(i, (i / 6) * SLOT);
    WAIT_V(0);
  }
  STAMP(5); CSTAMP(60);
  BAR();
  STAMP(32);
  u32 w2a[2];
  w2a[0] = v4 * 12288 + (8 * lh + lq) * 128 + (4 * ((lg ^ v4) & 1) + ((lp & 1) << 1) + (lp >> 1)) * 8 + (((lq >> 1) ^ (v4 >> 1)) & 1) * 64;
  w2a[1] = w2a[0] ^ 64;
  u32x4 wr[28][2];
  auto w2_rd = [&](int i) AI { const int k = i >> 1; const u32 a = w2a[i & 1] + (k < 24 ? ((k / 6) & 1) * SLOT : 0) + (k % 6) * 2048; wr[k][i & 1] = cat(lds_tr(a), lds_tr(a + 512)); };
  sfor<0, 8>([&](auto I) AI { kt_rd(I); });
  SCHED();
  sfor<0, 8>([&](auto I) AI { kt_mma(I); w2_rd(I); SCHED(); });
  sfor<8, 12>([&](auto I) AI { w2_rd(I); });
  SCHED();
  kt_put();
  STAMP(34);
  BAR();
  STAMP(35);
  sfor<0, 4>([&](auto I) AI { kt_fin(I); });
  sfor<0, 6>([&](auto I) AI {
    SCHED();
    if (I < 3) kt_fin(4 + I);
    if (I == 4) kt_fin(7);
    w2_rd(12 + 2 * I); w2_rd(13 + 2 * I);
    w2_piece(12 + I, 0);
  });
  STAMP(39);
  WAIT_V(6);
  STAMP(40);
  __builtin_amdgcn_raw_buffer_store_b64(u32x2{t32, (u32)(tag >> 32)}, mk_rsrc(P.ws + WS_FH, is_aux ? 0 : 2048 * FS), lane == 0 ? (u32)(((mb * 2 + th) * 64 + nb * 4 + fq) * FS) : OOB, 0, 16);
  BAR();
  STAMP(6);
  rsrc_t rh = mk_rsrc(P.ws + WS_H, H_BYTES);
  const u32 hso = (u32)(((tt * 2 + ch) * 112 + v4 * 28) * 1024);
  u32x4 hf[28];
  auto load_h = [&](int i) AI { hf[i] = ld16_async(rh, lane * 16, hso + i * 1024); };
  const int trow = tt * 64 + ch * 32 + (lane & 31), dcol = nb2 * 64 + 16 * v4 + 8 * lh;
  u32x4 xres;
  u32x2 uu[2];
  f32x4 csc;
  {
    f32x4 ya[8];
#pragma unroll
    for (int T = 0; T < 8; ++T) ya[T] = f32x4{0.f, 0.f, 0.f, 0.f};
    const int s00 = 16 * (lg >> 1) + lc - 8 * (lg & 1) - 7;
    const int par = s00 & 1;
    const u32 abase = LDS_U + ch * U_CH + par * U_COPY + 32 + (s00 - par) * 2 + v4 * 128;
    const u32 bbase = LDS_KX + ch * KX_STRIDE + 512 + (16 * (lc - (lg >> 1)) + 8 * (lg & 1)) * 2 - v4 * 128 - 64;
    auto rdA = [&](int ph, int mu) AI {
      const u32 a = abase + ph * 64 + mu * 512;
      return u32x4{lds_ld<u32>(a), lds_ld<u32>(a + 4), lds_ld<u32>(a + 8), lds_ld<u32>(a + 12)};
    };
    const char* pfh = P.ws + WS_FH + (tt * 64 + 14 * v4 + (lane < 14 ? lane : 0)) * FS;
    u64 fh = 0;
    u32x4 B[8], A[3];
    A[0] = rdA(0, 0);
#pragma unroll
    for (int g = 7; g >= 0; --g) B[g] = lds_ld<u32x4>(bbase + 64 + g * 512);
    A[1] = rdA(0, 1);
    sfor<0, 16>([&](auto I) AI {
      constexpr int st = I, ph = st >> 3, mu = st & 7;
      SCHED();
      if (st + 2 < 16) A[(st + 2) % 3] = rdA((st + 2) >> 3, (st + 2) & 7);
      sfor<0, 8 - mu>([&](auto J) AI {
        constexpr int T = 7 - J, c = 36 * ph + mu * 8 - mu * (mu - 1) / 2 + J;
        SCHED();
        ya[T] = mma_h(A[st % 3], B[T - mu], ya[T]);
        SCHED();
        if (T == 7 && ph == 0) B[7 - mu] = lds_ld<u32x4>(bbase + (7 - mu) * 512);
        if (c < 30 && c % 5 == 2) w2_piece(18 + c / 5, SLOT);
        if (c == 33) asm volatile("buffer_load_dwordx4 %0, %1, %2, 0 offen" : "=v"(xres) : "v"((u32)((trow * D + dcol) * 2)), "s"(rx) : "memory");
        if (c == POLLC) fh = ld8_async(pfh);
        if (c == CHKC) {
          STAMP(41);
          WAIT_V(0);
          PIN(fh);
          if (b == 255 && w == 7) {
            const char* p = P.ws + WS_FH + (lg * 128 + (lc < 14 ? lc : 0) * 4) * FS;
#pragma unroll 1
            for (u32 sp = 0; sp < SPIN_CAP; ++sp) {
              const u64 v0 = ld_agent(p), v1 = ld_agent(p + 512 * FS), v2 = ld_agent(p + 1024 * FS), v3 = ld_agent(p + 1536 * FS);
              if (all_of(((v0 ^ tag) | (v1 ^ tag) | (v2 ^ tag) | (v3 ^ tag)) == 0)) break;
              __builtin_amdgcn_s_sleep(1);
            }
            if (lane == 0) st_agent(P.ws + WS_EP, tag);
          }
          if (!all_of(fh == tag)) wait_flags(pfh, tag);
          STAMP(8);
        }
        if (c >= CHKC && c - CHKC < HW) load_h(c - CHKC);
      });
      SCHED();
      if (st == 12) {
#pragma unroll
        for (int i = 0; i < 2; ++i) uu[i] = lds_ld<u32x2>(LDS_U + i * U_CH + 32 + (256 * w + 16 * lc + 4 * lg) * 2);
        csc = lds_ld<f32x4>(LDS_SC);
      }
      if (st == 13) BAR();
      if (st >= 13) {
#pragma unroll
        for (int T = (st == 13 ? 0 : mu - 1); T < mu; ++T) {
          lds_st<f32x4>(SCR + (T * 8 + w) * 1024 + lane * 16, ya[T]);
        }
      }
    });
    STAMP(7);
    lds_st<f32x4>(SCR + (7 * 8 + w) * 1024 + lane * 16, ya[7]);
#pragma unroll
    for (int i = 72 - CHKC; i < HW; ++i) load_h(i);
    BAR();
  }
  STAMP(9); CSTAMP(61);
  {
    f32x16 oa[2];
#pragma unroll
    for (int i = 0; i < 16; ++i) { oa[0][i] = 0.f; oa[1][i] = 0.f; }
    rsrc_t ra = mk_rsrc(P.ws + WS_AO, AO_BYTES);
    u32x2 ao[4];
    const u32 aoff = (u32)((dcol >> 1) * AO_ROW + (trow >> 8) * 2048 + ((trow >> 1) & 1) * 1024 + ((trow >> 2) & 63) * 16 + (trow & 1) * 8);
    f32x4 y[8];
    sfor<0, 28>([&](auto I) AI {
      constexpr int s = I;
      if (HW + s < 28) load_h(HW + s);
      asm volatile("s_waitcnt vmcnt(%1)" : "+v"(hf[s]) : "n"(g2_wait_hf(s)) : "memory");
      if (s == 0) STAMP(46);
      if (s == 5) { STAMP(48); BAR(); STAMP(49); }
      if (s == 24 - PD) { STAMP(50); WAIT_V(G2_E); BAR(); w2a[0] += 2 * SLOT; w2a[1] += 2 * SLOT; PIN2(w2a[0], w2a[1]); STAMP(51); }
#pragma unroll
      for (int i = 0; i < 2; ++i) {
        SCHED();
        oa[i] = mma32(wr[s][i], hf[s], oa[i]);
        SCHED();
        if (s < 2) {
          y[4 * s + 2 * i] = lds_ld<f32x4>(SCR + (w * 8 + 4 * s + 2 * i) * 1024 + lane * 16);
          y[4 * s + 2 * i + 1] = lds_ld<f32x4>(SCR + (w * 8 + 4 * s + 2 * i + 1) * 1024 + lane * 16);
        }
        if (s == 2) {
          y[i] = (y[4 * i] + y[4 * i + 1]) + (y[4 * i + 2] + y[4 * i + 3]);
#pragma unroll
          for (int r = 0; r < 4; ++r) y[i][r] = FMA(y[i][r], csc[2 * i], h2f((uu[i][r >> 1] >> (16 * (r & 1))) & 0xffff) * csc[2 * i + 1]);
          PIN(y[i]);
        }
        if (s == 3)
          __builtin_amdgcn_raw_buffer_store_b128(u32x4{pk_bf16(y[0][2 * i], y[1][2 * i]), t32, pk_bf16(y[0][2 * i + 1], y[1][2 * i + 1]), t32}, ra,
                                                 b * AO_ROW + w * 2048 + (lc * 4 + lg) * 16 + i * 1024, 0, 16);
        if (s >= 5 && s <= 8 && i == 1) w2_piece(24 + s - 5, 2 * SLOT);
        if (s >= 20 && s <= 23 && i == 0)
          asm volatile("buffer_load_dwordx2 %0, %1, %2, %3 offen sc1" : "=v"(ao[s - 20]) : "v"(aoff), "s"(ra), "s"((s - 20) * AO_ROW) : "memory");
        if (s + PD >= 12 && s + PD < 28) w2_rd(2 * (s + PD) + i);
      }
    });
    STAMP(10);
#pragma unroll
    for (int g = 1; g < 4; ++g) {
      const u32 a = v4 * 12288 + ch * 6144 + lane * 16 + (g - 1) * 2048;
      const f32x16& A = oa[g >> 1];
      const int o = (g & 1) * 8;
      lds_st<f32x4>(a, f32x4{A[o], A[o + 1], A[o + 2], A[o + 3]});
      lds_st<f32x4>(a + 1024, f32x4{A[o + 4], A[o + 5], A[o + 6], A[o + 7]});
    }
    WAIT_V(0);
    PIN(xres);
#pragma unroll
    for (int e = 0; e < 4; ++e) PIN(ao[e]);
    auto ok = [&]() AI { return all_of(((ao[0][1] ^ t32) | (ao[1][1] ^ t32) | (ao[2][1] ^ t32) | (ao[3][1] ^ t32)) == 0); };
    if (__builtin_expect(!ok(), 0)) {
#pragma unroll 1
      for (u32 sp = 0; sp < SPIN_CAP; ++sp) {
#pragma unroll
        for (int e = 0; e < 4; ++e) ao[e] = __builtin_amdgcn_raw_buffer_load_b64(ra, aoff, e * AO_ROW, 16);
        if (ok()) break;
        __builtin_amdgcn_s_sleep(1);
        asm volatile("" ::: "memory");
      }
    }
    STAMP(52);
    float r[8];
#pragma unroll
    for (int e = 0; e < 4; ++e) {
      r[2 * e] = oa[0][2 * e] + (bf2f(xres[e]) + bf2f(ao[e][0])); r[2 * e + 1] = oa[0][2 * e + 1] + (bfhi(xres[e]) + bfhi(ao[e][0]));
      PIN2(r[2 * e], r[2 * e + 1]);
    }
    BAR();
    f32x4 p[6];
#pragma unroll
    for (int g = 1; g < 4; ++g) {
      const u32 a = (v4 ^ g) * 12288 + ch * 6144 + lane * 16 + (g - 1) * 2048;
      p[2 * g - 2] = lds_ld<f32x4>(a); p[2 * g - 1] = lds_ld<f32x4>(a + 1024);
    }
    SCHED();
#pragma unroll
    for (int g = 0; g < 3; ++g)
#pragma unroll
      for (int e = 0; e < 4; ++e) { r[e] += p[2 * g][e]; r[4 + e] += p[2 * g + 1][e]; }
    __builtin_amdgcn_raw_buffer_store_b128(u32x4{pk_bf16(r[0], r[1]), pk_bf16(r[2], r[3]), pk_bf16(r[4], r[5]), pk_bf16(r[6], r[7])}, mk_rsrc(P.out, L * D * 2), (trow * D + dcol) * 2, 0, 0);
    STAMP(11); CSTAMP(62);
    STAMP_OUT;
  }
}

#if defined(__HIP_DEVICE_COMPILE__)
#pragma clang attribute pop
#endif
extern "C" void kernel_launch(void* const* d_in, const int* in_sizes, int n_in,
                              void* d_out, int out_size, void* d_ws, size_t ws_size,
                              hipStream_t stream) {
  Params P;
  P.x = (const u16*)d_in[0]; P.lar = (const u16*)d_in[1]; P.aim = (const u16*)d_in[2];
  P.cre = (const u16*)d_in[3]; P.cim = (const u16*)d_in[4]; P.ldt = (const u16*)d_in[5];
  P.dsk = (const u16*)d_in[6]; P.w1 = (const u16*)d_in[8]; P.w3 = (const u16*)d_in[9];
  P.w2 = (const u16*)d_in[10]; P.out = (u16*)d_out; P.ws = (char*)d_ws;
  (void)hipFuncSetAttribute((const void*)fused, hipFuncAttributeMaxDynamicSharedMemorySize, LDS_TOTAL);
  fused<<<256, 512, LDS_TOTAL, stream>>>(P);
}
```

```cpp
#include <hip/hip_runtime.h>
#include <stdint.h>
#define PKEPI 0
#if defined(__HIP_DEVICE_COMPILE__) && !PKEPI
#pragma clang attribute push (__attribute__((target("no-packed-fp32-ops"))), apply_to = function)
#endif

typedef unsigned short u16;
typedef unsigned int u32;
typedef unsigned long long u64;
typedef __attribute__((ext_vector_type(4))) short s16x4;
typedef __attribute__((ext_vector_type(8))) __bf16 bf16x8;
typedef __attribute__((ext_vector_type(8))) _Float16 f16x8;
typedef __attribute__((ext_vector_type(2))) __bf16 bf16x2;
typedef __attribute__((ext_vector_type(2))) _Float16 f16x2;
typedef __attribute__((ext_vector_type(16))) float f32x16;
typedef __attribute__((ext_vector_type(4))) float f32x4;
typedef __attribute__((ext_vector_type(2))) float f32x2;
typedef __attribute__((ext_vector_type(4))) u32 u32x4;
typedef __attribute__((ext_vector_type(2))) u32 u32x2;
#define LAS __attribute__((address_space(3)))
#define GAS __attribute__((address_space(1)))
#define DEV __device__ __forceinline__
typedef __amdgpu_buffer_rsrc_t rsrc_t;

static constexpr int L = 2048, D = 512, NS = 128, F = 1792;

static constexpr size_t WS_H = 0;
static constexpr size_t WS_XT = 8u << 20;
static constexpr size_t WS_AO = 11u << 20;
static constexpr size_t WS_ST = 16u << 20;
static constexpr size_t WS_CS = WS_ST + (64u << 10);
static constexpr int FS = 128;
static constexpr size_t WS_FX = WS_ST + (128u << 10);
static constexpr size_t WS_FH = WS_FX + 32 * FS;
static constexpr size_t WS_EP = WS_FH + 2048 * FS;
static constexpr int H_BYTES = L * F * 2;
static constexpr int XT_BLK = 65536 + 256, XT_BYTES = 32 * XT_BLK;
static constexpr int AO_ROW = 16384 + 128, AO_BYTES = 256 * AO_ROW;
static constexpr u32 OOB = 0x7fffffffu;

static constexpr int SLOT = 49152;
static constexpr int LDS_TOTAL = 163840;
static constexpr int SCR = 2 * SLOT;
static constexpr int LDS_SC = LDS_TOTAL - 64;
static constexpr int KG_ROW = 112, KG_PLANE = 128 * KG_ROW;
static constexpr int LDS_KD = SCR + 4 * KG_PLANE;
static constexpr int U_COPY = 4160;
static constexpr int U_CH = 2 * U_COPY;
static constexpr int LDS_U = SCR;
static constexpr int KX_STRIDE = 4608;
static constexpr int LDS_KX = SCR + 2 * U_CH;

struct Params {
  const u16 *x, *lar, *aim, *cre, *cim, *ldt, *dsk, *w1, *w3, *w2;
  u16* out;
  char* ws;
};

DEV float asf(u32 v) { return __builtin_bit_cast(float, v); }
DEV u32 asu(float v) { return __builtin_bit_cast(u32, v); }
DEV float bf2f(u32 v) { return asf(v << 16); }
DEV float bfhi(u32 v) { return asf(v & 0xffff0000u); }
DEV u32 pk_bf16(float a, float b) { f32x2 p = {a, b}; return __builtin_bit_cast(u32, __builtin_convertvector(p, bf16x2)); }
DEV u32 pk_f16(float a, float b) { f32x2 p = {a, b}; return __builtin_bit_cast(u32, __builtin_convertvector(p, f16x2)); }
DEV float rbf(float a) { return bf2f(pk_bf16(a, a)); }
DEV float h2f(u32 v) { return (float)__builtin_bit_cast(_Float16, (u16)v); }
DEV bool all_of(bool p) { return __builtin_amdgcn_ballot_w64(!p) == 0; }
#define FMA __builtin_fmaf
#define AI __attribute__((always_inline))
template <int I> struct IC { constexpr operator int() const { return I; } };
template <int B, int E, typename FN> DEV void sfor(FN&& f) { if constexpr (B < E) { f(IC<B>{}); sfor<B + 1, E>(f); } }

template <typename T> DEV T lds_ld(u32 a) { return *(const LAS T*)a; }
template <typename T> DEV void lds_st(u32 a, T v) { *(LAS T*)a = v; }
DEV u32x2 lds_tr(u32 a) { return __builtin_bit_cast(u32x2, __builtin_amdgcn_ds_read_tr16_b64_v4i16((LAS s16x4*)a)); }
DEV u32x4 cat(u32x2 a, u32x2 b) { return __builtin_shufflevector(a, b, 0, 1, 2, 3); }
DEV f32x16 mma32(u32x4 a, u32x4 b, f32x16 c) { return __builtin_amdgcn_mfma_f32_32x32x16_bf16(__builtin_bit_cast(bf16x8, a), __builtin_bit_cast(bf16x8, b), c, 0, 0, 0); }
DEV f32x4 mma_bf(u32x4 a, u32x4 b, f32x4 c) { return __builtin_amdgcn_mfma_f32_16x16x32_bf16(__builtin_bit_cast(bf16x8, a), __builtin_bit_cast(bf16x8, b), c, 0, 0, 0); }
DEV f32x4 mma_h(u32x4 a, u32x4 b, f32x4 c) { return __builtin_amdgcn_mfma_f32_16x16x32_f16(__builtin_bit_cast(f16x8, a), __builtin_bit_cast(f16x8, b), c, 0, 0, 0); }
DEV rsrc_t mk_rsrc(const void* p, u32 bytes) { return __builtin_amdgcn_make_buffer_rsrc((void*)p, 0, bytes, 0x00020000); }
DEV void dma16(rsrc_t rs, u32 voff, u32 sbase, int simm, u32 lbase, int limm) {
  u32 t;
  asm volatile("s_add_u32 m0, %1, %2\n\ts_add_u32 %0, %3, %4\n\tbuffer_load_dwordx4 %5, %6, %0 offen lds"
               : "=&s"(t) : "s"(lbase), "n"(limm), "s"(sbase), "n"(simm), "v"(voff), "s"(rs) : "memory", "scc");
}
#define PINS(x) asm volatile("" : "+s"(x))
#define WAIT_V(n) asm volatile("s_waitcnt vmcnt(%0)" :: "n"(n) : "memory")
#define BAR() asm volatile("s_waitcnt lgkmcnt(0)\n\ts_barrier" ::: "memory")
#define SCHED() __builtin_amdgcn_sched_barrier(0)
#define PIN(x) asm volatile("" : "+v"(x))
#define PIN2(x, y) asm volatile("" : "+v"(x), "+v"(y))
#define RLX __ATOMIC_RELAXED, __HIP_MEMORY_SCOPE_AGENT
DEV u64 ld_agent(const char* p) { return __hip_atomic_load((GAS u64*)p, RLX); }
DEV void st_agent(char* p, u64 v) { __hip_atomic_store((GAS u64*)p, v, RLX); }
static constexpr u32 CODE_BYTES = 23 * 1024;
#define PFW 0
static constexpr u32 SPIN_CAP = 1u << 16;

#define STAMP(i) do {} while (0)
#define STAMP_DECL
#define CSTAMP(i)
#define STAMP_OUT

DEV void wait_flags(const char* p, u64 tag) {
#pragma unroll 1
  for (u32 s = 0; s < SPIN_CAP; ++s) {
    u64 v = ld_agent(p);
    if (all_of(v == tag)) break;
    __builtin_amdgcn_s_sleep(1);
  }
  asm volatile("" ::: "memory");
}
DEV u64 ld8_async(const char* p) {
  u64 v;
  asm volatile("global_load_dwordx2 %0, %1, off sc1" : "=v"(v) : "v"(p) : "memory");
  return v;
}
DEV u32x4 ld16_async(rsrc_t rs, u32 voff, u32 soff) {
  u32x4 v;
  asm volatile("buffer_load_dwordx4 %0, %1, %2, %3 offen sc1" : "=v"(v) : "v"(voff), "s"(rs), "s"(soff) : "memory");
  return v;
}
DEV u32 ld4_async(const u16* base, u32 off) {
  u32 v;
  asm volatile("global_load_dword %0, %1, %2" : "=v"(v) : "v"(off), "s"(base) : "memory");
  return v;
}
template <int C> DEV float dppf(float v) { return asf((u32)__builtin_amdgcn_update_dpp(0, (int)asu(v), C, 0xf, 0xf, true)); }
DEV float row_sum(float v) { v += dppf<0x128>(v); v += dppf<0x124>(v); v += dppf<0x122>(v); v += dppf<0x121>(v); return v; }
DEV float row_max(float v) { v = __builtin_fmaxf(v, dppf<0x128>(v)); v = __builtin_fmaxf(v, dppf<0x124>(v)); v = __builtin_fmaxf(v, dppf<0x122>(v)); v = __builtin_fmaxf(v, dppf<0x121>(v)); return v; }
DEV float fold32(float a, float b) {
  auto r = __builtin_amdgcn_permlane32_swap(asu(a), asu(b), false, false);
  return asf(r[0]) + asf(r[1]);
}
DEV float fold16(float a, float b) {
  auto r = __builtin_amdgcn_permlane16_swap(asu(a), asu(b), false, false);
  return asf(r[0]) + asf(r[1]);
}
DEV float dot2bf(u32 a, u32 b, float c) { return __builtin_amdgcn_fdot2_f32_bf16(__builtin_bit_cast(bf16x2, a), __builtin_bit_cast(bf16x2, b), c, false); }
DEV u32 rdu(float v, int l) { return (u32)__builtin_amdgcn_readlane((int)asu(v), l); }
DEV float rdl(float v, int l) { return asf(rdu(v, l)); }
DEV u32 umax(u32 a, u32 b) { return a > b ? a : b; }
DEV u64 mk_tag(u32& misc) {
  PIN(misc);
  const u32 lo = (u32)__builtin_amdgcn_readlane((int)misc, 2), hi = (u32)__builtin_amdgcn_readlane((int)misc, 3);
  return (((u64)hi << 32) | lo) + 0x9E3779B97F4A7C15ull;
}

struct cpx { float r, i; };
DEV cpx cmul(cpx a, cpx b) { return cpx{FMA(a.r, b.r, -(a.i * b.i)), FMA(a.r, b.i, a.i * b.r)}; }
DEV cpx csqr(cpx a) { return cpx{FMA(a.r, a.r, -(a.i * a.i)), (a.r + a.r) * a.i}; }
DEV cpx cexp_at(float ar2, float airev, float p) {
  float e = __builtin_amdgcn_exp2f(ar2 * p);
  float fr = __builtin_amdgcn_fractf(airev * p);
  return cpx{e * __builtin_amdgcn_cosf(fr), e * __builtin_amdgcn_sinf(fr)};
}
struct KgIn { u32 lar, aim, cr, ci, misc; };
struct KgSeed { cpx v[32], m[3], qa[2], qb[2], C, s1, w512; float inv, dsk, dt, ar2, airev, mx, sm; };
struct KdTab { u16 v[2048]; };
constexpr KdTab make_kd() {
  KdTab t{};
  for (int i = 0; i < 2048; ++i) {
    int l = (i & ~7) + 7 - (i & 7);
    if (l == 0) l = 1;
    int sh = l < 256 ? 0 : l < 512 ? 1 : l < 1024 ? 2 : 3;
    int q = l >> sh, rem = l - (q << sh), half = (1 << sh) >> 1;
    if (sh > 0 && (rem > half || (rem == half && (q & 1)))) ++q;
    int p = (q << sh) - 1;
    int I = p >> 7, sg = I < 2 ? 0 : I < 4 ? 1 : I < 8 ? 2 : 3;
    int r = 8 * sg + (I << (3 - sg)) + ((p & 7) >> sg);
    t.v[i] = (u16)((r * 16 + ((p >> 3) & 15)) * 2);
  }
  return t;
}
__constant__ __attribute__((aligned(16))) KdTab kd_tab = make_kd();

#define HW 14
static constexpr int PD = 4;
constexpr int g2_ops_at(int s) { return (HW + s < 28 ? 1 : 0) + (s == 3 ? 2 : 0) + (s >= 5 && s <= 8 ? 1 : 0) + (s >= 20 && s <= 23 ? 1 : 0); }
constexpr int g2_issued_through(int s) { int n = HW; for (int i = 0; i <= s; ++i) n += g2_ops_at(i); return n; }
constexpr int g2_pos_hf(int i) { return i < HW ? i + 1 : g2_issued_through(i - HW - 1) + 1; }
constexpr int g2_wait_hf(int s) { return g2_issued_through(s - 1) + (HW + s < 28 ? 1 : 0) - g2_pos_hf(s); }
static constexpr int G2_E = g2_issued_through(23 - PD) + (HW + 24 - PD < 28 ? 1 : 0) - g2_issued_through(8);

__global__ __launch_bounds__(512, 2) void fused(Params P) {
  const int b = __builtin_amdgcn_workgroup_id_x(), xcd = b & 7, idx = b >> 3;
  const int tid = __builtin_amdgcn_workitem_id_x(), lane = tid & 63;
  const int w = __builtin_amdgcn_readfirstlane(tid >> 6);
  STAMP_DECL;
  STAMP(0); CSTAMP(58);
  {
    u64 pc;
    asm volatile("s_getpc_b64 %0" : "=s"(pc));
    dma16(mk_rsrc((const void*)(pc & ~(u64)255), CODE_BYTES), w == PFW ? idx * 1024 + lane * 16 : OOB, 0, 0, 3 * SLOT, 0);
  }
  KgIn kin;
  const bool is_aux = idx >= 28;
  const int mb = (xcd & 3) * 4 + (idx & 3), nb = (xcd >> 2) * 7 + (idx >> 2);
  const int tt = xcd * 4 + (idx & 3), nb2 = idx >> 2;
  const int th = w & 1, fq = w >> 1;
  const int ch = w >> 2, v4 = w & 3;
  const int lc = lane & 15, lg = lane >> 4, lq = (lane & 15) >> 2, lp = lane & 3, lh = lane >> 5;
  rsrc_t rx = mk_rsrc(P.x, L * D * 2);
  rsrc_t rw = mk_rsrc(w >= 4 ? P.w3 : P.w1, D * F * 2);
  const u32 vx0 = (lane >> 3) * 1024 + (((lane & 7) ^ lg) * 16);
  const u32 vx1 = vx0 ^ 64;
  const u32 vw = lg * (F * 2) + ((lc ^ (lg << 2)) * 16);
  const u32 sx0 = (u32)((mb * 128 + 16 * w) * 1024), sw0 = (u32)(16 * (w & 3) * (F * 2) + nb * 256);
  const u32 lx0 = w * 2048, lw0 = w * 4096;
  auto g1_piece = [&](int s, int i) AI {
    const int slot = ((s + 1) % 3) * SLOT;
    if (i == 0) dma16(rx, vx0, sx0, s * 128, lx0, slot);
    else if (i == 1) dma16(rx, vx1, sx0, s * 128 + 8192, lx0, slot + 1024);
    else dma16(rw, vw, sw0, (s * 64 + (i - 2) * 4) * F * 2, lw0, slot + 16384 + (i - 2) * 1024);
  };
  auto small_loads = [&](int k) AI {
    const u32 o = (2 * b + ch) * (NS * 2) + lane * 4;
    if (k == 0) {
      const char* p = lane == 0 ? (const char*)(P.ldt + 2 * b) : lane == 1 ? (const char*)(P.dsk + 2 * b) : P.ws + WS_EP + (lane & 1) * 4;
      asm volatile("global_load_dword %0, %1, off sc1" : "=v"(kin.misc) : "v"(p) : "memory");
    }
    if (k == 1) { kin.lar = ld4_async(P.lar, o); kin.aim = ld4_async(P.aim, o); }
    if (k == 2) { kin.cr = ld4_async(P.cre, o); kin.ci = ld4_async(P.cim, o); }
  };
  u64 tag; u32 t32;
  KgSeed kg;
  rsrc_t rw2 = mk_rsrc(P.w2, F * D * 2);
  const u32 vw2 = (lane >> 3) * 1024 + (((lane & 7) ^ ((lg & 1) << 2)) * 16);
  u32 sw2 = (u32)((v4 * 448 + ch * 8) * 1024 + nb2 * 128), lw2 = v4 * 12288 + ch * 1024;
  PINS(sw2); PINS(lw2);
  auto w2_piece = [&](int k, int slot) AI { dma16(rw2, vw2, sw2, k * 16384, lw2, slot + (k % 6) * 2048); };
  rsrc_t rxt = mk_rsrc(P.ws + WS_XT, XT_BYTES);

  auto pinc = [&](cpx& c) AI { PIN2(c.r, c.i); };
  auto sel = [&](cpx a, cpx b2) AI { return cpx{th ? a.r : b2.r, th ? a.i : b2.i}; };
  auto seed_piece = [&](int k) AI {
    const u32 msh = ((w >> 1) & 1) ? 0 : 16, csh = ch ? 0 : 16;
    if (k == 0) {
      asm volatile("" : "+v"(kin.lar), "+v"(kin.aim), "+v"(kin.cr), "+v"(kin.ci));
      kg.dt = rbf(__builtin_amdgcn_exp2f(bfhi((u32)__builtin_amdgcn_readlane((int)kin.misc, 0) << csh) * 1.44269504088896f)); PIN(kg.dt);
    }
    if (k == 1) { kg.ar2 = (-rbf(__builtin_amdgcn_exp2f(bfhi(kin.lar << msh) * 1.44269504088896f)) * kg.dt) * 1.44269504088896f; PIN(kg.ar2); }
    if (k == 2) {
      kg.airev = (bfhi(kin.aim << msh) * kg.dt) * 0.159154943091895f;
      kg.C = cpx{bfhi(kin.cr << msh), bfhi(kin.ci << msh)}; PIN(kg.airev); pinc(kg.C);
    }
    if (k == 3) { kg.mx = dot2bf(kin.ci & 0x7fff7fffu, 0x3f803f80u, dot2bf(kin.cr & 0x7fff7fffu, 0x3f803f80u, 0.f)); kg.sm = dot2bf(kin.cr, 0x3f803f80u, 0.f); PIN2(kg.mx, kg.sm); }
    if (k == 4) { kg.sm = fold32(kg.sm, kg.mx); kg.sm = fold16(kg.sm, kg.sm); }
    if (k == 5) { kg.sm += dppf<0x128>(kg.sm); kg.sm += dppf<0x124>(kg.sm); }
    if (k == 6) { kg.sm += dppf<0x122>(kg.sm); kg.sm += dppf<0x121>(kg.sm); kg.sm *= 2.f * kg.dt; }
    if (k >= 4 && k <= 6) PIN(kg.sm);
    if (k == 7) {
      kg.dsk = bfhi((u32)__builtin_amdgcn_readlane((int)kin.misc, 1) << csh) + rdl(kg.sm, 0);
      const u32 se = 268u - ((rdu(kg.sm, 32) >> 23) & 0xff);
      kg.mx = asf((se > 254u ? 254u : se) << 23); PIN(kg.dsk);
    }
    if (k == 8) {
      kg.inv = asf((254u << 23) - asu(kg.mx));
      const float cs = 2.f * kg.dt * kg.mx;
      kg.C.r *= cs; kg.C.i *= cs; PIN(kg.inv); pinc(kg.C);
    }
    if (k == 9) { kg.qa[1] = cexp_at(kg.ar2, kg.airev, 128.f); pinc(kg.qa[1]); }
    if (k == 10) { kg.qb[1] = csqr(kg.qa[1]); pinc(kg.qb[1]); }
    if (k == 11) { kg.w512 = csqr(kg.qb[1]); kg.qb[0] = sel(kg.qa[1], kg.w512); pinc(kg.qb[0]); }
    if (k == 12) { kg.s1 = cexp_at(kg.ar2, kg.airev, th ? 1.f : 8.f); kg.s1.i = th ? kg.s1.i : -kg.s1.i; pinc(kg.s1); }
    if (k == 13) { kg.m[0] = csqr(kg.s1); pinc(kg.m[0]); }
    if (k == 14) { kg.m[1] = csqr(kg.m[0]); kg.qa[0] = sel(kg.m[1], kg.qb[1]); pinc(kg.m[1]); pinc(kg.qa[0]); }
    if (k == 15) { kg.m[2] = sel(kg.qa[1], csqr(kg.m[1])); pinc(kg.m[2]); }
    if (k == 16) { kg.v[0] = sel(cmul(kg.C, kg.s1), cpx{1.f, 0.f}); pinc(kg.v[0]); }
    if (k == 17) { kg.v[1] = cmul(kg.v[0], kg.s1); pinc(kg.v[1]); }
    if (k == 18) { kg.v[16] = cexp_at(kg.ar2, kg.airev, th ? 258.f : 1032.f); pinc(kg.v[16]); }
    if (k == 19) { kg.v[16] = cmul(kg.v[16], kg.C); pinc(kg.v[16]); }
    if (k == 20) { kg.v[17] = cmul(kg.v[16], sel(kg.m[0], kg.qa[1])); pinc(kg.v[17]); }
    if (k == 21) { kg.v[24] = cmul(kg.v[17], kg.qb[1]); kg.v[25] = cmul(kg.v[24], kg.m[1]); pinc(kg.v[24]); pinc(kg.v[25]); }
  };
  u32 kre[16], kim[16];
  auto kx_mul = [&](int d, int s, cpx m) AI { kg.v[d] = cmul(kg.v[s], m); PIN2(kg.v[d].r, kg.v[d].i); };
  auto kx_pack = [&](int p) AI { kre[p] = pk_f16(kg.v[2 * p].r, kg.v[2 * p + 1].r); kim[p] = pk_f16(kg.v[2 * p].i, kg.v[2 * p + 1].i); PIN2(kre[p], kim[p]); };
  auto kx_piece = [&](int k) AI {
    if (k < 20) {
      const int q = k / 10, j = k % 10, o = 16 + 8 * q;
      if (j < 2) kx_mul(o + 2 + j, o + j, kg.qa[q]);
      else if (j < 6) kx_mul(o + 2 + j, o + j - 2, kg.qb[q]);
      else kx_pack(o / 2 + j - 6);
    } else {
      const int j = k - 20;
      if (j < 2) kx_mul(2 + j, j, kg.m[0]);
      else if (j < 6) kx_mul(2 + j, j - 2, kg.m[1]);
      else {
        const int p = (j - 6) >> 2, r = (j - 6) & 3;
        if (r < 2) kx_mul(8 + 2 * p + r, 2 * p + r, kg.m[2]);
        else kx_pack(p + 4 * (r - 2));
      }
    }
  };
  auto kt_store = [&](int i) AI {
    const int c = i >> 1;
    u32 a = SCR + ch * 2 * KG_PLANE + (64 * ((w >> 1) & 1) + lane) * KG_ROW + (th ? c * 16 : (c < 2 ? 80 + c * 16 : 64)) + (i & 1) * KG_PLANE;
    if (c == 3) a = th ? a : LDS_KD + 4096 + lane * 16;
    const u32* p = (i & 1) ? kim : kre;
    lds_st<u32x4>(a, u32x4{p[4 * c], p[4 * c + 1], p[4 * c + 2], p[4 * c + 3]});
  };
  const int kt = v4 < 3 ? v4 : 0;
  const u32 krb = SCR + ch * 2 * KG_PLANE + (8 * lg + lq) * KG_ROW + lp * 8, kra = krb + kt * 32;
  u32x4 kA[8], kB[8];
  f32x4 ka = {0.f, 0.f, 0.f, 0.f}, kb = ka;
  auto kt_rd = [&](int k) AI {
    const u32 o = k * 32 * KG_ROW;
    kA[k] = cat(lds_tr(kra + o), lds_tr(kra + o + 4 * KG_ROW)); kB[k] = cat(lds_tr(krb + 80 + o), lds_tr(krb + 80 + o + 4 * KG_ROW));
  };
  auto kt_mma = [&](int k) AI { if (k & 1) kb = mma_h(kB[k], kA[k], kb); else ka = mma_h(kB[k], kA[k], ka); };
  auto kt_put = [&]() AI {
    ka += kb;
    lds_st<f32x2>(LDS_SC + ch * 8, f32x2{kg.inv, kg.dsk});
    lds_st<u32x2>(LDS_KD + ch * 1536 + ((16 * kt + lc) * 16 + 4 * lg) * 2, u32x2{pk_f16(ka[0], ka[1]), pk_f16(ka[2], ka[3])});
  };
  u32x4 uv, kdv; u32 unx, kgv[8];
  const int g8 = tid & 255;
  auto load_u = [&]() AI {
    const u32 cb = (2 * b + ch) * 128, gn = g8 + 1;
    uv = ld16_async(rxt, (g8 >> 3) * XT_BLK + (g8 & 7) * 16 + cb, 0);
    asm volatile("buffer_load_dword %0, %1, %2, 0 offen sc1" : "=v"(unx) : "v"((gn >> 3) * XT_BLK + (gn & 7) * 16 + cb), "s"(rxt) : "memory");
    asm volatile("global_load_dwordx4 %0, %1, %2" : "=v"(kdv) : "v"(g8 * 16), "s"(&kd_tab) : "memory");
  };
  auto kt_fin = [&](int k) AI {
    if (k == 0) { PIN(uv); PIN(unx); PIN(kdv); }
    if (k < 4) { kgv[2 * k] = lds_ld<u16>(LDS_KD + ch * 1536 + (kdv[k] & 0xffffu)); kgv[2 * k + 1] = lds_ld<u16>(LDS_KD + ch * 1536 + (kdv[k] >> 16)); }
    if (k == 4) lds_st<u32x4>(LDS_U + ch * U_CH + 32 + g8 * 16, uv);
    if (k == 5) lds_st<u32x4>(LDS_U + ch * U_CH + U_COPY + 32 + g8 * 16,
                              u32x4{__builtin_amdgcn_alignbit(uv[1], uv[0], 16), __builtin_amdgcn_alignbit(uv[2], uv[1], 16),
                                    __builtin_amdgcn_alignbit(uv[3], uv[2], 16), __builtin_amdgcn_alignbit(unx, uv[3], 16)});
    if (k == 6) {
      u32 za = LDS_U + ch * U_CH + ((g8 + 3) & 1) * 16 + (((g8 + 3) >> 1) & 1) * U_COPY, zb = LDS_KX + ch * KX_STRIDE + (g8 & 31) * 16;
      PIN2(za, zb);
      lds_st<u32x4>(g8 < 4 ? za : zb, u32x4{0, 0, 0, g8 == 0 ? uv[0] << 16 : 0u});
    }
    if (k == 7) {
#pragma unroll
      for (int j = 0; j < 8; ++j) PIN(kgv[j]);
      lds_st<u32x4>(LDS_KX + ch * KX_STRIDE + 512 + g8 * 16, u32x4{kgv[0] | (kgv[1] << 16), kgv[2] | (kgv[3] << 16), kgv[4] | (kgv[5] << 16), kgv[6] | (g8 == 0 ? 0u : kgv[7] << 16)});
    }
  };
  const char* pfx = P.ws + WS_FX + (4 * w + lp) * FS;

  f32x16 ag[2], au[2];
#pragma unroll
  for (int i = 0; i < 16; ++i) { ag[0][i] = 0.f; ag[1][i] = 0.f; au[0][i] = 0.f; au[1][i] = 0.f; }
  float eke[2] = {1.f, 1.f}, es2[2] = {1.f, 1.f};
  rsrc_t rhs = mk_rsrc(P.ws + WS_H, is_aux ? 0 : H_BYTES);
  const u32 ho = (u32)(((mb * 4 + th * 2) * 112 + nb * 8 + fq * 2) * 1024 + lane * 16);
  float ee[4], eg[4]; u32x4 eo;
  auto epi_piece = [&](int t2, int k) AI {
    const int q = k / 5, j = k % 5;
#pragma unroll
    for (int i = 0; i < 4; ++i) {
      if (j == 0) { ee[i] = ag[t2][4 * q + i] * eke[t2]; eg[i] = ag[t2][4 * q + i] * au[t2][4 * q + i]; }
      if (j == 1) ee[i] = __builtin_amdgcn_exp2f(ee[i]);
      if (j == 2) ee[i] = FMA(ee[i], es2[t2], es2[t2]);
      if (j == 3) ee[i] = __builtin_amdgcn_rcpf(ee[i]);
      if (j == 4) eg[i] *= ee[i];
    }
    if (j < 4) asm volatile("" : "+v"(ee[0]), "+v"(ee[1]), "+v"(ee[2]), "+v"(ee[3]), "+v"(eg[0]), "+v"(eg[1]), "+v"(eg[2]), "+v"(eg[3]));
    if (j == 4) {
      eo[2 * (q & 1)] = pk_bf16(eg[0], eg[1]); eo[2 * (q & 1) + 1] = pk_bf16(eg[2], eg[3]);
      if (q & 1) __builtin_amdgcn_raw_buffer_store_b128(eo, rhs, ho + (t2 * 112 + (q >> 1)) * 1024, 0, 16);
      else PIN2(eo[0], eo[1]);
    }
  };

  if (__builtin_expect(!is_aux, 1)) {
#pragma unroll
    for (int i = 0; i < 12; ++i) { if (i == 0) STAMP(12); g1_piece(i / 6, i % 6); if (i == 5) { STAMP(13); BAR(); } }
    STAMP(2); CSTAMP(59);
    u32 xa[4], wa;
    {
      const int row = 64 * th + (lane & 31);
#pragma unroll
      for (int ks = 0; ks < 4; ++ks) xa[ks] = SLOT + row * 128 + (((2 * ks + lh) ^ ((row >> 1) & 7)) * 16);
      wa = SLOT + 16384 + (8 * lh + lq) * 256 + ((fq ^ lq) * 64) + (4 * (lg & 1) + ((lp & 1) << 1) + (lp >> 1)) * 8;
    }
    const int G = (lane & 31) >> 2;
    const char* pcs = P.ws + WS_CS + (size_t)(lh * F + nb * 128 + fq * 32 + (4 * (G >> 2) + 2 * (G & 1) + ((G >> 1) & 1)) * 4 + lp) * 8;
    const char* pst = P.ws + WS_ST + (size_t)(mb * 128 + 64 * th + lane) * 8;
    u64 gc, gs, fx; u32 gst[2];
    u32x4 xg, xu, xm[2];
    WAIT_V(6);
    STAMP(15);
    BAR();
    STAMP(3);
    u32x4 fx0[4], fx1[4], fg[4], fu[4];
    auto rd0 = [&](int d, int ks) AI { fx0[d] = lds_ld<u32x4>(xa[ks]); fg[d] = cat(lds_tr(wa + ks * 4096), lds_tr(wa + ks * 4096 + 1024)); };
    auto rd1 = [&](int d, int ks) AI { fu[d] = cat(lds_tr(wa + 16384 + ks * 4096), lds_tr(wa + 16384 + ks * 4096 + 1024)); fx1[d] = lds_ld<u32x4>(xa[ks] + 4096); };
    rd0(0, 0); rd1(0, 0);
    g1_piece(2, 0); g1_piece(2, 1);
    auto filler = [&](int n) AI {
      if (n >= 9 && n < 12) small_loads(n - 9);
      if (n == 28) { tag = mk_tag(kin.misc); t32 = (u32)tag; }
      if (n >= 29 && n < 58 && n % 4 != 0) seed_piece(n - 29 - (n - 29) / 4);
      if (n >= 58 && n < 110 && n % 5 != 2) kx_piece(n - 58 - (n - 58) / 5);
      if (n == 92) fx = ld8_async(pfx);
      if (n == 93) { gc = ld8_async(pcs); gs = ld8_async(pst); }
      if (n == 120) {
        WAIT_V(10);
        PIN(gc); PIN(gs); PIN(fx);
        auto ok = [&]() AI { return all_of((((u32)(gc >> 32) ^ t32) | ((u32)(gs >> 32) ^ t32) | ((u32)fx ^ t32) | ((u32)(fx >> 32) ^ (u32)(tag >> 32))) == 0); };
        if (__builtin_expect(!ok(), 0)) {
#pragma unroll 1
          for (u32 sp = 0; sp < SPIN_CAP; ++sp) {
            gc = ld_agent(pcs); gs = ld_agent(pst); fx = ld_agent(pfx);
            if (ok()) break;
            __builtin_amdgcn_s_sleep(1);
          }
        }
      }
      if (n == 121) {
        const float cv = asf((u32)gc);
        const float c0 = rbf(cv), r1 = cv - c0, c1 = rbf(r1);
        const u32 e0 = pk_bf16(c0, c1), e1 = pk_bf16(r1 - c1, 0.f);
        xg = u32x4{lh ? 0u : e0, lh ? 0u : e1, 0, 0}; xu = u32x4{lh ? e0 : 0u, lh ? e1 : 0u, 0, 0};
        PIN2(xg, xu);
        auto r = __builtin_amdgcn_permlane32_swap((u32)gs, (u32)gs, false, false);
        gst[0] = r[0]; gst[1] = r[1];
      }
      if (n == 122 || n == 123) {
        const int t = n - 122;
        const u32 m = (gst[t] & 0xffffu) ^ 0x8000u;
        xm[t] = u32x4{m | (m << 16), m, 0, 0};
        const float sd = bfhi(gst[t]);
        eke[t] = -1.44269504088896f * __builtin_amdgcn_rcpf(sd); es2[t] = sd * sd;
        PIN(xm[t]); PIN2(eke[t], es2[t]);
      }
    };
    sfor<0, 28>([&](auto I) AI {
      constexpr int s = I / 4, ks = I % 4;
      if (ks == 3) {
        STAMP(16 + 2 * s);
        if (s == 0) WAIT_V(11); else if (s == 6) WAIT_V(9); else if (s < 7) WAIT_V(6);
        BAR();
        if (s < 7) {
          const int dl = (s + 2) % 3 == 0 ? -2 * SLOT : SLOT;
#pragma unroll
          for (int i = 0; i < 4; ++i) { xa[i] += dl; PIN(xa[i]); }
          wa += dl; PIN(wa);
        }
        STAMP(17 + 2 * s);
      }
      const int n = s * 16 + ks * 4, kn = (ks + 1) & 3;
      const int rs = ks == 3 ? s : s - 1, pi = ks == 3 ? 0 : 2 * ks + 2;
      auto piece = [&](int i) AI {
        if (i > 5) return;
        if (rs + 3 < 8) g1_piece(rs + 3, i);
        else if (rs == 5) w2_piece(i, 0);
        else if (rs == 6) w2_piece(6 + i, SLOT);
      };
      SCHED(); ag[0] = mma32(fg[0], fx0[0], ag[0]); SCHED();
      rd0(1, kn);
      filler(n);
      SCHED(); au[0] = mma32(fu[0], fx0[0], au[0]); SCHED();
      rd1(1, kn);
      filler(n + 1);
      SCHED(); ag[1] = mma32(fg[0], fx1[0], ag[1]); SCHED();
      filler(n + 2); piece(pi);
      SCHED(); au[1] = mma32(fu[0], fx1[0], au[1]); SCHED();
      filler(n + 3); piece(pi + 1);
      SCHED();
      fx0[0] = fx0[1]; fx1[0] = fx1[1]; fg[0] = fg[1]; fu[0] = fu[1];
    });
    sfor<0, 4>([&](auto I) AI {
      constexpr int ks = I;
      SCHED(); ag[0] = mma32(fg[ks], fx0[ks], ag[0]); SCHED();
      if (ks < 3) rd0(ks + 1, ks + 1);
      if (ks == 0) { w2_piece(8, SLOT); w2_piece(9, SLOT); }
      if (ks == 1) filler(120);
      if (ks == 2) filler(122);
      if (ks == 3) { STAMP(30); WAIT_V(6); BAR(); STAMP(31); load_u(); }
      SCHED(); au[0] = mma32(fu[ks], fx0[ks], au[0]); SCHED();
      if (ks < 3) rd1(ks + 1, ks + 1);
      if (ks == 1) { filler(121); w2_piece(10, SLOT); w2_piece(11, SLOT); }
      if (ks == 2) filler(123);
    });
    SCHED(); ag[0] = mma32(xg, xm[0], ag[0]); SCHED(); kt_store(0);
    SCHED(); au[0] = mma32(xu, xm[0], au[0]); SCHED(); kt_store(1);
    sfor<0, 10>([&](auto I) AI {
      constexpr int i = I;
      SCHED();
      if (i < 8) { if (i & 1) au[1] = mma32(fu[i >> 1], fx1[i >> 1], au[1]); else ag[1] = mma32(fg[i >> 1], fx1[i >> 1], ag[1]); }
      else if (i == 8) ag[1] = mma32(xg, xm[1], ag[1]); else au[1] = mma32(xu, xm[1], au[1]);
      SCHED();
      if (i < 6) kt_store(2 + i);
      if (i > 0) { epi_piece(0, 2 * i - 2); epi_piece(0, 2 * i - 1); }
    });
    epi_piece(0, 18); epi_piece(0, 19);
    STAMP(4);
  } else {
    const int a = xcd * 4 + idx - 28, ca = a < 28 ? a : 27;
    const int cmat = ca / 14, cnb = ca % 14;
    rsrc_t rc = mk_rsrc(cmat == 1 ? P.w3 : P.w1, D * F * 2);
    const u32 vo = (tid >> 4) * (F * 2) + cnb * 256 + lc * 16;
    u32x4 wv[16], xv[8];
    float s1[8], s2[8];
#pragma unroll
    for (int i = 0; i < 8; ++i) xv[i] = __builtin_amdgcn_raw_buffer_load_b128(rx, lane * 16, (64 * a + 8 * w + i) * 1024, 0);
    small_loads(0); small_loads(1); small_loads(2);
    WAIT_V(4);
    tag = mk_tag(kin.misc); t32 = (u32)tag;
#pragma unroll
    for (int i = 0; i < 8; ++i) {
      s1[i] = 0.f; s2[i] = 0.f;
#pragma unroll
      for (int j = 0; j < 4; ++j) { s1[i] = dot2bf(xv[i][j], 0x3f803f80u, s1[i]); s2[i] = dot2bf(xv[i][j], xv[i][j], s2[i]); }
      wv[i] = __builtin_amdgcn_raw_buffer_load_b128(rc, vo, i * 32 * F * 2, 0);
      SCHED();
    }
    float rs[2], mr[2];
#pragma unroll
    for (int g = 0; g < 2; ++g) {
      const float S = row_sum(fold16(fold32(s1[4 * g], s1[4 * g + 2]), fold32(s1[4 * g + 1], s1[4 * g + 3])));
      const float Q = row_sum(fold16(fold32(s2[4 * g], s2[4 * g + 2]), fold32(s2[4 * g + 1], s2[4 * g + 3])));
      const float mf = S * (1.f / D);
      const float var = rbf(__builtin_fmaxf(Q * (1.f / D) - mf * mf, 0.f));
      const float m = rbf(mf);
      const float sd = rbf(__builtin_amdgcn_sqrtf(rbf(var + 1e-5f)));
      rs[g] = __builtin_amdgcn_rcpf(sd);
      mr[g] = -m * rs[g];
      __builtin_amdgcn_raw_buffer_store_b64(u32x2{asu(sd) | (asu(m) >> 16), t32}, mk_rsrc(P.ws + WS_ST, L * 8), lc == 0 ? (u32)(64 * a + 8 * w + 4 * g + lg) * 8 : OOB, 0, 16);
    }
    float rr[8], cc[8];
#pragma unroll
    for (int i = 0; i < 8; ++i) { rr[i] = rdl(rs[i >> 2], (i & 3) * 16); cc[i] = rdl(mr[i >> 2], (i & 3) * 16); }
#pragma unroll
    for (int e = 0; e < 8; ++e) {
      u32x4 o;
#pragma unroll
      for (int j = 0; j < 4; ++j) {
        const u32 v0 = xv[2 * j][e >> 1], v1 = xv[2 * j + 1][e >> 1];
        o[j] = pk_f16(FMA((e & 1) ? bfhi(v0) : bf2f(v0), rr[2 * j], cc[2 * j]), FMA((e & 1) ? bfhi(v1) : bf2f(v1), rr[2 * j + 1], cc[2 * j + 1]));
      }
      lds_st<u32x4>((u32)((8 * lane + e) * 128 + ((w ^ (lane & 7)) * 16)), o);
      wv[8 + e] = __builtin_amdgcn_raw_buffer_load_b128(rc, vo, (8 + e) * 32 * F * 2, 0);
      SCHED();
    }
    BAR();
#pragma unroll
    for (int j = 0; j < 8; ++j) {
      const int d = (tid >> 3) + 64 * j, sl = tid & 7;
      __builtin_amdgcn_raw_buffer_store_b128(lds_ld<u32x4>((u32)(d * 128 + ((sl ^ ((d >> 3) & 7)) * 16))), rxt, a * XT_BLK + d * 128 + sl * 16, 0, 16);
    }
    u32x4 sel;
    const u32 one = (lc & 1) ? 0x3f800000u : 0x3f80u;
    sel[0] = (lc >> 1) == 0 ? one : 0; sel[1] = (lc >> 1) == 1 ? one : 0;
    sel[2] = (lc >> 1) == 2 ? one : 0; sel[3] = (lc >> 1) == 3 ? one : 0;
    f32x4 c0 = {0.f, 0.f, 0.f, 0.f}, c1 = c0;
#pragma unroll
    for (int k = 0; k < 16; k += 2) { c0 = mma_bf(sel, wv[k], c0); c1 = mma_bf(sel, wv[k + 1], c1); }
    if (lane < 32) lds_st<f32x4>(65536 + w * 512 + lc * 32 + lg * 16, c0 + c1);
    WAIT_V(0);
    BAR();
    if (tid == 0) st_agent(P.ws + WS_FX + a * FS, tag);
    if (tid < 128) {
      float sum = 0.f;
#pragma unroll
      for (int j = 0; j < 8; ++j) sum += lds_ld<float>(65536 + j * 512 + tid * 4);
      st_agent(P.ws + WS_CS + (size_t)(cmat * F + cnb * 128 + tid) * 8, ((u64)t32 << 32) | asu(sum));
    }
    STAMP(2); CSTAMP(59);
#pragma unroll
    for (int k = 0; k < 22; ++k) seed_piece(k);
#pragma unroll
    for (int k = 0; k < 42; ++k) kx_piece(k);
#pragma unroll
    for (int i = 0; i < 8; ++i) kt_store(i);
    STAMP(17);
    wait_flags(pfx, tag);
    BAR();
    load_u();
#pragma unroll
    for (int i = 0; i < 12; ++i) w2_piece(i, (i / 6) * SLOT);
    WAIT_V(0);
  }
  STAMP(5); CSTAMP(60);
  BAR();
  STAMP(32);
  u32 w2a[2];
  w2a[0] = v4 * 12288 + (8 * lh + lq) * 128 + (4 * ((lg ^ v4) & 1) + ((lp & 1) << 1) + (lp >> 1)) * 8 + (((lq >> 1) ^ (v4 >> 1)) & 1) * 64;
  w2a[1] = w2a[0] ^ 64;
  u32x4 wr[28][2];
  auto w2_rd = [&](int i) AI { const int k = i >> 1; const u32 a = w2a[i & 1] + (k < 24 ? ((k / 6) & 1) * SLOT : 0) + (k % 6) * 2048; wr[k][i & 1] = cat(lds_tr(a), lds_tr(a + 512)); };
  sfor<0, 20>([&](auto I) AI {
    constexpr int k = I;
    SCHED();
    if (k < 8) kt_rd(k); else w2_rd(k - 8);
    if (k >= 3 && k < 11) kt_mma(k - 3);
    if (k == 14) kt_put();
    SCHED();
    epi_piece(1, k);
  });
  STAMP(34);
  WAIT_V(4);
  BAR();
  STAMP(35);
  sfor<0, 8>([&](auto I) AI { kt_fin(I); });
  STAMP(39);
  WAIT_V(0);
  STAMP(40);
  __builtin_amdgcn_raw_buffer_store_b64(u32x2{t32, (u32)(tag >> 32)}, mk_rsrc(P.ws + WS_FH, is_aux ? 0 : 2048 * FS), lane == 0 ? (u32)(((mb * 2 + th) * 64 + nb * 4 + fq) * FS) : OOB, 0, 16);
  BAR();
  STAMP(6);
  rsrc_t rh = mk_rsrc(P.ws + WS_H, H_BYTES);
  const u32 hso = (u32)(((tt * 2 + ch) * 112 + v4 * 28) * 1024);
  u32x4 hf[28];
  auto load_h = [&](int i) AI { hf[i] = ld16_async(rh, lane * 16, hso + i * 1024); };
  const int trow = tt * 64 + ch * 32 + (lane & 31), dcol = nb2 * 64 + 16 * v4 + 8 * lh;
  u32x4 xres;
  u32x2 uu[2];
  f32x4 csc;
  {
    f32x4 ya[8];
#pragma unroll
    for (int T = 0; T < 8; ++T) ya[T] = f32x4{0.f, 0.f, 0.f, 0.f};
    const int s00 = 16 * (lg >> 1) + lc - 8 * (lg & 1) - 7;
    const int par = s00 & 1;
    const u32 abase = LDS_U + ch * U_CH + par * U_COPY + 32 + (s00 - par) * 2 + v4 * 128;
    const u32 bbase = LDS_KX + ch * KX_STRIDE + 512 + (16 * (lc - (lg >> 1)) + 8 * (lg & 1)) * 2 - v4 * 128 - 64;
    auto rdA = [&](int ph, int mu) AI {
      const u32 a = abase + ph * 64 + mu * 512;
      return u32x4{lds_ld<u32>(a), lds_ld<u32>(a + 4), lds_ld<u32>(a + 8), lds_ld<u32>(a + 12)};
    };
    const char* pfh = P.ws + WS_FH + (tt * 64 + 14 * v4 + (lane < 14 ? lane : 0)) * FS;
    u64 fh = 0;
    u32x4 B[8], A[3];
    csc = lds_ld<f32x4>(LDS_SC);
    auto put_y = [&](int T) AI { lds_st<f32x4>(SCR + ((T ^ 4) * 8 + w) * 1024 + lane * 16, ya[T]); };
    A[0] = rdA(0, 0);
#pragma unroll
    for (int g = 7; g >= 0; --g) B[g] = lds_ld<u32x4>(bbase + 64 + g * 512);
    A[1] = rdA(0, 1);
    sfor<0, 16>([&](auto I) AI {
      constexpr int st = I, ph = st >> 3, mu = st & 7;
      SCHED();
      if (st + 2 < 16) A[(st + 2) % 3] = rdA((st + 2) >> 3, (st + 2) & 7);
      sfor<0, 8 - mu>([&](auto J) AI {
        constexpr int T = 7 - J, c = 36 * ph + mu * 8 - mu * (mu - 1) / 2 + J;
        SCHED();
        ya[T] = mma_h(A[st % 3], B[T - mu], ya[T]);
        SCHED();
        if (T == 7 && ph == 0) B[7 - mu] = lds_ld<u32x4>(bbase + (7 - mu) * 512);
        if (c < 24 && (c & 1)) w2_rd(12 + (c >> 1));
        if (c < 24 && c % 4 == 0) w2_piece(12 + c / 4, 0);
        if (c == 29) BAR();
        if (c >= 30 && c < 42 && !(c & 1)) w2_piece(18 + (c - 30) / 2, SLOT);
        if (c == 25) asm volatile("buffer_load_dwordx4 %0, %1, %2, 0 offen" : "=v"(xres) : "v"((u32)((trow * D + dcol) * 2)), "s"(rx) : "memory");
        if (c == POLLC) fh = ld8_async(pfh);
        if (c == CHKC) {
          STAMP(41);
          WAIT_V(0);
          PIN(fh);
          if (b == 255 && w == 7) {
            const char* p = P.ws + WS_FH + (lg * 128 + (lc < 14 ? lc : 0) * 4) * FS;
#pragma unroll 1
            for (u32 sp = 0; sp < SPIN_CAP; ++sp) {
              const u64 v0 = ld_agent(p), v1 = ld_agent(p + 512 * FS), v2 = ld_agent(p + 1024 * FS), v3 = ld_agent(p + 1536 * FS);
              if (all_of(((v0 ^ tag) | (v1 ^ tag) | (v2 ^ tag) | (v3 ^ tag)) == 0)) break;
              __builtin_amdgcn_s_sleep(1);
            }
            if (lane == 0) st_agent(P.ws + WS_EP, tag);
          }
          if (!all_of(fh == tag)) wait_flags(pfh, tag);
          STAMP(8);
        }
        if (c >= CHKC && c - CHKC < HW) load_h(c - CHKC);
      });
      SCHED();
      if (st == 12) {
#pragma unroll
        for (int i = 0; i < 2; ++i) uu[i] = lds_ld<u32x2>(LDS_U + i * U_CH + 32 + (256 * w + 16 * lc + 4 * lg) * 2);
      }
      if (st == 13) BAR();
      if (st >= 9) put_y(mu - 1);
    });
    STAMP(7);
    put_y(7);
#pragma unroll
    for (int i = 72 - CHKC; i < HW; ++i) load_h(i);
    BAR();
  }
  STAMP(9); CSTAMP(61);
  {
    f32x16 oa[2];
#pragma unroll
    for (int i = 0; i < 16; ++i) { oa[0][i] = 0.f; oa[1][i] = 0.f; }
    rsrc_t ra = mk_rsrc(P.ws + WS_AO, AO_BYTES);
    u32x2 ao[4];
    const u32 aoff = (u32)((dcol >> 1) * AO_ROW + (trow >> 8) * 2048 + ((trow >> 1) & 1) * 1024 + ((trow >> 2) & 63) * 16 + (trow & 1) * 8);
    f32x4 y[8];
    sfor<0, 24>([&](auto I) AI {
      constexpr int s = I;
      if (HW + s < 28) load_h(HW + s);
      asm volatile("s_waitcnt vmcnt(%1)" : "+v"(hf[s]) : "n"(g2_wait_hf(s)) : "memory");
      if (s == 0) STAMP(46);
      if (s == 5) { STAMP(48); BAR(); STAMP(49); }
      if (s == 24 - PD) { STAMP(50); WAIT_V(G2_E); BAR(); w2a[0] += 2 * SLOT; w2a[1] += 2 * SLOT; PIN2(w2a[0], w2a[1]); STAMP(51); }
#pragma unroll
      for (int i = 0; i < 2; ++i) {
        SCHED();
        oa[i] = mma32(wr[s][i], hf[s], oa[i]);
        SCHED();
        if (s < 2) {
          y[4 * s + 2 * i] = lds_ld<f32x4>(SCR + ((w ^ 4) * 8 + 4 * s + 2 * i) * 1024 + lane * 16);
          y[4 * s + 2 * i + 1] = lds_ld<f32x4>(SCR + ((w ^ 4) * 8 + 4 * s + 2 * i + 1) * 1024 + lane * 16);
        }
        if (s == 2) {
          y[i] = (y[4 * i] + y[4 * i + 1]) + (y[4 * i + 2] + y[4 * i + 3]);
#pragma unroll
          for (int r = 0; r < 4; ++r) y[i][r] = FMA(y[i][r], csc[2 * i], h2f((uu[i][r >> 1] >> (16 * (r & 1))) & 0xffff) * csc[2 * i + 1]);
          PIN(y[i]);
        }
        if (s == 3)
          __builtin_amdgcn_raw_buffer_store_b128(u32x4{pk_bf16(y[0][2 * i], y[1][2 * i]), t32, pk_bf16(y[0][2 * i + 1], y[1][2 * i + 1]), t32}, ra,
                                                 b * AO_ROW + w * 2048 + (lc * 4 + lg) * 16 + i * 1024, 0, 16);
        if (s >= 5 && s <= 8 && i == 1) w2_piece(24 + s - 5, 2 * SLOT);
        if (s >= 20 && s <= 23 && i == 0)
          asm volatile("buffer_load_dwordx2 %0, %1, %2, %3 offen sc1" : "=v"(ao[s - 20]) : "v"(aoff), "s"(ra), "s"((s - 20) * AO_ROW) : "memory");
        if (s + PD >= 12 && s + PD < 28) w2_rd(2 * (s + PD) + i);
      }
    });
    auto put_o = [&](int j) AI {
      const int g = 1 + (j >> 1), o = (g & 1) * 8 + (j & 1) * 4;
      const f32x16& A = oa[g >> 1];
      lds_st<f32x4>(v4 * 12288 + ch * 6144 + lane * 16 + j * 1024, f32x4{A[o], A[o + 1], A[o + 2], A[o + 3]});
    };
    asm volatile("s_waitcnt vmcnt(4)" : "+v"(hf[24]), "+v"(hf[25]), "+v"(hf[26]), "+v"(hf[27]) :: "memory");
    sfor<24, 28>([&](auto I) AI { oa[1] = mma32(wr[I][1], hf[I], oa[1]); });
    sfor<24, 28>([&](auto I) AI { SCHED(); oa[0] = mma32(wr[I][0], hf[I], oa[0]); SCHED(); if (I > 24) put_o(I - 23); });
    put_o(5);
    STAMP(10);
    put_o(0); put_o(1);
    WAIT_V(0);
    PIN(xres);
#pragma unroll
    for (int e = 0; e < 4; ++e) PIN(ao[e]);
    auto ok = [&]() AI { return all_of(((ao[0][1] ^ t32) | (ao[1][1] ^ t32) | (ao[2][1] ^ t32) | (ao[3][1] ^ t32)) == 0); };
    if (__builtin_expect(!ok(), 0)) {
#pragma unroll 1
      for (u32 sp = 0; sp < SPIN_CAP; ++sp) {
#pragma unroll
        for (int e = 0; e < 4; ++e) ao[e] = __builtin_amdgcn_raw_buffer_load_b64(ra, aoff, e * AO_ROW, 16);
        if (ok()) break;
        __builtin_amdgcn_s_sleep(1);
        asm volatile("" ::: "memory");
      }
    }
    STAMP(52);
    float r[8];
#pragma unroll
    for (int e = 0; e < 4; ++e) {
      r[2 * e] = oa[0][2 * e] + (bf2f(xres[e]) + bf2f(ao[e][0])); r[2 * e + 1] = oa[0][2 * e + 1] + (bfhi(xres[e]) + bfhi(ao[e][0]));
      PIN2(r[2 * e], r[2 * e + 1]);
    }
    BAR();
    f32x4 p[6];
#pragma unroll
    for (int g = 1; g < 4; ++g) {
      const u32 a = (v4 ^ g) * 12288 + ch * 6144 + lane * 16 + (g - 1) * 2048;
      p[2 * g - 2] = lds_ld<f32x4>(a); p[2 * g - 1] = lds_ld<f32x4>(a + 1024);
    }
    SCHED();
#pragma unroll
    for (int g = 0; g < 3; ++g)
#pragma unroll
      for (int e = 0; e < 4; ++e) { r[e] += p[2 * g][e]; r[4 + e] += p[2 * g + 1][e]; }
    __builtin_amdgcn_raw_buffer_store_b128(u32x4{pk_bf16(r[0], r[1]), pk_bf16(r[2], r[3]), pk_bf16(r[4], r[5]), pk_bf16(r[6], r[7])}, mk_rsrc(P.out, L * D * 2), (trow * D + dcol) * 2, 0, 0);
    STAMP(11); CSTAMP(62);
    STAMP_OUT;
  }
}

#if defined(__HIP_DEVICE_COMPILE__) && !PKEPI
#pragma clang attribute pop
#endif
extern "C" void kernel_launch(void* const* d_in, const int* in_sizes, int n_in,
                              void* d_out, int out_size, void* d_ws, size_t ws_size,
                              hipStream_t stream) {
  Params P;
  P.x = (const u16*)d_in[0]; P.lar = (const u16*)d_in[1]; P.aim = (const u16*)d_in[2];
  P.cre = (const u16*)d_in[3]; P.cim = (const u16*)d_in[4]; P.ldt = (const u16*)d_in[5];
  P.dsk = (const u16*)d_in[6]; P.w1 = (const u16*)d_in[8]; P.w3 = (const u16*)d_in[9];
  P.w2 = (const u16*)d_in[10]; P.out = (u16*)d_out; P.ws = (char*)d_ws;
  (void)hipFuncSetAttribute((const void*)fused, hipFuncAttributeMaxDynamicSharedMemorySize, LDS_TOTAL);
  fused<<<256, 512, LDS_TOTAL, stream>>>(P);
}
```

```cpp
#include <hip/hip_runtime.h>
#include <stdint.h>
#define PKEPI 0
#if defined(__HIP_DEVICE_COMPILE__) && !PKEPI
#pragma clang attribute push (__attribute__((target("no-packed-fp32-ops"))), apply_to = function)
#endif

typedef unsigned short u16;
typedef unsigned int u32;
typedef unsigned long long u64;
typedef __attribute__((ext_vector_type(4))) short s16x4;
typedef __attribute__((ext_vector_type(8))) __bf16 bf16x8;
typedef __attribute__((ext_vector_type(8))) _Float16 f16x8;
typedef __attribute__((ext_vector_type(2))) __bf16 bf16x2;
typedef __attribute__((ext_vector_type(2))) _Float16 f16x2;
typedef __attribute__((ext_vector_type(16))) float f32x16;
typedef __attribute__((ext_vector_type(4))) float f32x4;
typedef __attribute__((ext_vector_type(2))) float f32x2;
typedef __attribute__((ext_vector_type(4))) u32 u32x4;
typedef __attribute__((ext_vector_type(2))) u32 u32x2;
#define LAS __attribute__((address_space(3)))
#define GAS __attribute__((address_space(1)))
#define DEV __device__ __forceinline__
typedef __amdgpu_buffer_rsrc_t rsrc_t;

static constexpr int L = 2048, D = 512, NS = 128, F = 1792;

static constexpr size_t WS_H = 0;
static constexpr size_t WS_XT = 8u << 20;
static constexpr size_t WS_AO = 11u << 20;
static constexpr size_t WS_ST = 16u << 20;
static constexpr size_t WS_CS = WS_ST + (64u << 10);
static constexpr int FS = 128;
static constexpr size_t WS_FX = WS_ST + (128u << 10);
static constexpr size_t WS_FH = WS_FX + 32 * FS;
static constexpr size_t WS_EP = WS_FH + 2048 * FS;
static constexpr int H_BYTES = L * F * 2;
static constexpr int XT_BLK = 65536 + 256, XT_BYTES = 32 * XT_BLK;
static constexpr int AO_ROW = 16384 + 128, AO_BYTES = 256 * AO_ROW;
static constexpr u32 OOB = 0x7fffffffu;

static constexpr int SLOT = 49152;
static constexpr int LDS_TOTAL = 163840;
static constexpr int SCR = 2 * SLOT;
static constexpr int LDS_SC = LDS_TOTAL - 64;
static constexpr int KG_ROW = 112, KG_PLANE = 128 * KG_ROW;
static constexpr int LDS_KD = SCR + 4 * KG_PLANE;
static constexpr int U_COPY = 4160;
static constexpr int U_CH = 2 * U_COPY;
static constexpr int LDS_U = SCR;
static constexpr int KX_STRIDE = 4608;
static constexpr int LDS_KX = SCR + 2 * U_CH;

struct Params {
  const u16 *x, *lar, *aim, *cre, *cim, *ldt, *dsk, *w1, *w3, *w2;
  u16* out;
  char* ws;
};

DEV float asf(u32 v) { return __builtin_bit_cast(float, v); }
DEV u32 asu(float v) { return __builtin_bit_cast(u32, v); }
DEV float bf2f(u32 v) { return asf(v << 16); }
DEV float bfhi(u32 v) { return asf(v & 0xffff0000u); }
DEV u32 pk_bf16(float a, float b) { f32x2 p = {a, b}; return __builtin_bit_cast(u32, __builtin_convertvector(p, bf16x2)); }
DEV u32 pk_f16(float a, float b) { f32x2 p = {a, b}; return __builtin_bit_cast(u32, __builtin_convertvector(p, f16x2)); }
DEV float rbf(float a) { return bf2f(pk_bf16(a, a)); }
DEV float h2f(u32 v) { return (float)__builtin_bit_cast(_Float16, (u16)v); }
DEV bool all_of(bool p) { return __builtin_amdgcn_ballot_w64(!p) == 0; }
#define FMA __builtin_fmaf
#define AI __attribute__((always_inline))
template <int I> struct IC { constexpr operator int() const { return I; } };
template <int B, int E, typename FN> DEV void sfor(FN&& f) { if constexpr (B < E) { f(IC<B>{}); sfor<B + 1, E>(f); } }

template <typename T> DEV T lds_ld(u32 a) { return *(const LAS T*)a; }
template <typename T> DEV void lds_st(u32 a, T v) { *(LAS T*)a = v; }
DEV u32x2 lds_tr(u32 a) { return __builtin_bit_cast(u32x2, __builtin_amdgcn_ds_read_tr16_b64_v4i16((LAS s16x4*)a)); }
DEV u32x4 cat(u32x2 a, u32x2 b) { return __builtin_shufflevector(a, b, 0, 1, 2, 3); }
DEV f32x16 mma32(u32x4 a, u32x4 b, f32x16 c) { return __builtin_amdgcn_mfma_f32_32x32x16_bf16(__builtin_bit_cast(bf16x8, a), __builtin_bit_cast(bf16x8, b), c, 0, 0, 0); }
DEV f32x4 mma_bf(u32x4 a, u32x4 b, f32x4 c) { return __builtin_amdgcn_mfma_f32_16x16x32_bf16(__builtin_bit_cast(bf16x8, a), __builtin_bit_cast(bf16x8, b), c, 0, 0, 0); }
DEV f32x4 mma_h(u32x4 a, u32x4 b, f32x4 c) { return __builtin_amdgcn_mfma_f32_16x16x32_f16(__builtin_bit_cast(f16x8, a), __builtin_bit_cast(f16x8, b), c, 0, 0, 0); }
DEV rsrc_t mk_rsrc(const void* p, u32 bytes) { return __builtin_amdgcn_make_buffer_rsrc((void*)p, 0, bytes, 0x00020000); }
DEV void dma16(rsrc_t rs, u32 voff, u32 sbase, int simm, u32 lbase, int limm) {
  u32 t;
  asm volatile("s_add_u32 m0, %1, %2\n\ts_add_u32 %0, %3, %4\n\tbuffer_load_dwordx4 %5, %6, %0 offen lds"
               : "=&s"(t) : "s"(lbase), "n"(limm), "s"(sbase), "n"(simm), "v"(voff), "s"(rs) : "memory", "scc");
}
#define PINS(x) asm volatile("" : "+s"(x))
#define WAIT_V(n) asm volatile("s_waitcnt vmcnt(%0)" :: "n"(n) : "memory")
#define BAR() asm volatile("s_waitcnt lgkmcnt(0)\n\ts_barrier" ::: "memory")
#define SCHED() __builtin_amdgcn_sched_barrier(0)
#define PIN(x) asm volatile("" : "+v"(x))
#define PIN2(x, y) asm volatile("" : "+v"(x), "+v"(y))
#define RLX __ATOMIC_RELAXED, __HIP_MEMORY_SCOPE_AGENT
DEV u64 ld_agent(const char* p) { return __hip_atomic_load((GAS u64*)p, RLX); }
DEV void st_agent(char* p, u64 v) { __hip_atomic_store((GAS u64*)p, v, RLX); }
static constexpr u32 CODE_BYTES = 0;
#define PFW 0
#define R4(x) x x x x
#define R16(x) R4(R4(x))
#define R256(x) R16(R16(x))
static constexpr u32 SPIN_CAP = 1u << 16;

#define STAMP(i) do {} while (0)
#define STAMP_DECL
#define CSTAMP(i)
#define STAMP_OUT

DEV void wait_flags(const char* p, u64 tag) {
#pragma unroll 1
  for (u32 s = 0; s < SPIN_CAP; ++s) {
    u64 v = ld_agent(p);
    if (all_of(v == tag)) break;
    __builtin_amdgcn_s_sleep(1);
  }
  asm volatile("" ::: "memory");
}
DEV u64 ld8_async(const char* p) {
  u64 v;
  asm volatile("global_load_dwordx2 %0, %1, off sc1" : "=v"(v) : "v"(p) : "memory");
  return v;
}
DEV u32x4 ld16_async(rsrc_t rs, u32 voff, u32 soff) {
  u32x4 v;
  asm volatile("buffer_load_dwordx4 %0, %1, %2, %3 offen sc1" : "=v"(v) : "v"(voff), "s"(rs), "s"(soff) : "memory");
  return v;
}
DEV u32 ld4_async(const u16* base, u32 off) {
  u32 v;
  asm volatile("global_load_dword %0, %1, %2" : "=v"(v) : "v"(off), "s"(base) : "memory");
  return v;
}
template <int C> DEV float dppf(float v) { return asf((u32)__builtin_amdgcn_update_dpp(0, (int)asu(v), C, 0xf, 0xf, true)); }
DEV float row_sum(float v) { v += dppf<0x128>(v); v += dppf<0x124>(v); v += dppf<0x122>(v); v += dppf<0x121>(v); return v; }
DEV float row_max(float v) { v = __builtin_fmaxf(v, dppf<0x128>(v)); v = __builtin_fmaxf(v, dppf<0x124>(v)); v = __builtin_fmaxf(v, dppf<0x122>(v)); v = __builtin_fmaxf(v, dppf<0x121>(v)); return v; }
DEV float fold32(float a, float b) {
  auto r = __builtin_amdgcn_permlane32_swap(asu(a), asu(b), false, false);
  return asf(r[0]) + asf(r[1]);
}
DEV float fold16(float a, float b) {
  auto r = __builtin_amdgcn_permlane16_swap(asu(a), asu(b), false, false);
  return asf(r[0]) + asf(r[1]);
}
DEV float dot2bf(u32 a, u32 b, float c) { return __builtin_amdgcn_fdot2_f32_bf16(__builtin_bit_cast(bf16x2, a), __builtin_bit_cast(bf16x2, b), c, false); }
DEV u32 rdu(float v, int l) { return (u32)__builtin_amdgcn_readlane((int)asu(v), l); }
DEV float rdl(float v, int l) { return asf(rdu(v, l)); }
DEV u32 umax(u32 a, u32 b) { return a > b ? a : b; }
DEV u64 mk_tag(u32& misc) {
  PIN(misc);
  const u32 lo = (u32)__builtin_amdgcn_readlane((int)misc, 2), hi = (u32)__builtin_amdgcn_readlane((int)misc, 3);
  return (((u64)hi << 32) | lo) + 0x9E3779B97F4A7C15ull;
}

struct cpx { float r, i; };
DEV cpx cmul(cpx a, cpx b) { return cpx{FMA(a.r, b.r, -(a.i * b.i)), FMA(a.r, b.i, a.i * b.r)}; }
DEV cpx csqr(cpx a) { return cpx{FMA(a.r, a.r, -(a.i * a.i)), (a.r + a.r) * a.i}; }
DEV cpx cexp_at(float ar2, float airev, float p) {
  float e = __builtin_amdgcn_exp2f(ar2 * p);
  float fr = __builtin_amdgcn_fractf(airev * p);
  return cpx{e * __builtin_amdgcn_cosf(fr), e * __builtin_amdgcn_sinf(fr)};
}
struct KgIn { u32 lar, aim, cr, ci, misc; };
struct KgSeed { cpx v[32], m[3], qa[2], qb[2], C, s1, w512; float inv, dsk, dt, ar2, airev, mx, sm; };
struct KdTab { u16 v[2048]; };
constexpr KdTab make_kd() {
  KdTab t{};
  for (int i = 0; i < 2048; ++i) {
    int l = (i & ~7) + 7 - (i & 7);
    if (l == 0) l = 1;
    int sh = l < 256 ? 0 : l < 512 ? 1 : l < 1024 ? 2 : 3;
    int q = l >> sh, rem = l - (q << sh), half = (1 << sh) >> 1;
    if (sh > 0 && (rem > half || (rem == half && (q & 1)))) ++q;
    int p = (q << sh) - 1;
    int I = p >> 7, sg = I < 2 ? 0 : I < 4 ? 1 : I < 8 ? 2 : 3;
    int r = 8 * sg + (I << (3 - sg)) + ((p & 7) >> sg);
    t.v[i] = (u16)((r * 16 + ((p >> 3) & 15)) * 2);
  }
  return t;
}
__constant__ __attribute__((aligned(16))) KdTab kd_tab = make_kd();

#define HW 14
static constexpr int PD = 4;
constexpr int g2_ops_at(int s) { return (HW + s < 28 ? 1 : 0) + (s == 3 ? 2 : 0) + (s >= 5 && s <= 8 ? 1 : 0) + (s >= 20 && s <= 23 ? 1 : 0); }
constexpr int g2_issued_through(int s) { int n = HW; for (int i = 0; i <= s; ++i) n += g2_ops_at(i); return n; }
constexpr int g2_pos_hf(int i) { return i < HW ? i + 1 : g2_issued_through(i - HW - 1) + 1; }
constexpr int g2_wait_hf(int s) { return g2_issued_through(s - 1) + (HW + s < 28 ? 1 : 0) - g2_pos_hf(s); }
static constexpr int G2_E = g2_issued_through(23 - PD) + (HW + 24 - PD < 28 ? 1 : 0) - g2_issued_through(8);

__global__ __launch_bounds__(512, 2) void fused(Params P) {
  const int b = __builtin_amdgcn_workgroup_id_x(), xcd = b & 7, idx = b >> 3;
  const int tid = __builtin_amdgcn_workitem_id_x(), lane = tid & 63;
  const int w = __builtin_amdgcn_readfirstlane(tid >> 6);
  STAMP_DECL;
  STAMP(0); CSTAMP(58);
  {
    u64 pc;
    asm volatile("s_getpc_b64 %0" : "=s"(pc));
    dma16(mk_rsrc((const void*)(pc & ~(u64)255), CODE_BYTES), w == PFW ? idx * 1024 + lane * 16 : OOB, 0, 0, 3 * SLOT, 0);
  }
  KgIn kin;
  const bool is_aux = idx >= 28;
  const int mb = (xcd & 3) * 4 + (idx & 3), nb = (xcd >> 2) * 7 + (idx >> 2);
  const int tt = xcd * 4 + (idx & 3), nb2 = idx >> 2;
  const int th = w & 1, fq = w >> 1;
  const int ch = w >> 2, v4 = w & 3;
  const int lc = lane & 15, lg = lane >> 4, lq = (lane & 15) >> 2, lp = lane & 3, lh = lane >> 5;
  rsrc_t rx = mk_rsrc(P.x, L * D * 2);
  rsrc_t rw = mk_rsrc(w >= 4 ? P.w3 : P.w1, D * F * 2);
  const u32 vx0 = (lane >> 3) * 1024 + (((lane & 7) ^ lg) * 16);
  const u32 vx1 = vx0 ^ 64;
  const u32 vw = lg * (F * 2) + ((lc ^ (lg << 2)) * 16);
  const u32 sx0 = (u32)((mb * 128 + 16 * w) * 1024), sw0 = (u32)(16 * (w & 3) * (F * 2) + nb * 256);
  const u32 lx0 = w * 2048, lw0 = w * 4096;
  auto g1_piece = [&](int s, int i) AI {
    const int slot = ((s + 1) % 3) * SLOT;
    if (i == 0) dma16(rx, vx0, sx0, s * 128, lx0, slot);
    else if (i == 1) dma16(rx, vx1, sx0, s * 128 + 8192, lx0, slot + 1024);
    else dma16(rw, vw, sw0, (s * 64 + (i - 2) * 4) * F * 2, lw0, slot + 16384 + (i - 2) * 1024);
  };
  auto small_loads = [&](int k) AI {
    const u32 o = (2 * b + ch) * (NS * 2) + lane * 4;
    if (k == 0) {
      const char* p = lane == 0 ? (const char*)(P.ldt + 2 * b) : lane == 1 ? (const char*)(P.dsk + 2 * b) : P.ws + WS_EP + (lane & 1) * 4;
      asm volatile("global_load_dword %0, %1, off sc1" : "=v"(kin.misc) : "v"(p) : "memory");
    }
    if (k == 1) { kin.lar = ld4_async(P.lar, o); kin.aim = ld4_async(P.aim, o); }
    if (k == 2) { kin.cr = ld4_async(P.cre, o); kin.ci = ld4_async(P.cim, o); }
  };
  u64 tag; u32 t32;
  KgSeed kg;
  rsrc_t rw2 = mk_rsrc(P.w2, F * D * 2);
  const u32 vw2 = (lane >> 3) * 1024 + (((lane & 7) ^ ((lg & 1) << 2)) * 16);
  u32 sw2 = (u32)((v4 * 448 + ch * 8) * 1024 + nb2 * 128), lw2 = v4 * 12288 + ch * 1024;
  PINS(sw2); PINS(lw2);
  auto w2_piece = [&](int k, int slot) AI { dma16(rw2, vw2, sw2, k * 16384, lw2, slot + (k % 6) * 2048); };
  rsrc_t rxt = mk_rsrc(P.ws + WS_XT, XT_BYTES);

  auto pinc = [&](cpx& c) AI { PIN2(c.r, c.i); };
  auto sel = [&](cpx a, cpx b2) AI { return cpx{th ? a.r : b2.r, th ? a.i : b2.i}; };
  auto seed_piece = [&](int k) AI {
    const u32 msh = ((w >> 1) & 1) ? 0 : 16, csh = ch ? 0 : 16;
    if (k == 0) {
      asm volatile("" : "+v"(kin.lar), "+v"(kin.aim), "+v"(kin.cr), "+v"(kin.ci));
      kg.dt = rbf(__builtin_amdgcn_exp2f(bfhi((u32)__builtin_amdgcn_readlane((int)kin.misc, 0) << csh) * 1.44269504088896f)); PIN(kg.dt);
    }
    if (k == 1) { kg.ar2 = (-rbf(__builtin_amdgcn_exp2f(bfhi(kin.lar << msh) * 1.44269504088896f)) * kg.dt) * 1.44269504088896f; PIN(kg.ar2); }
    if (k == 2) {
      kg.airev = (bfhi(kin.aim << msh) * kg.dt) * 0.159154943091895f;
      kg.C = cpx{bfhi(kin.cr << msh), bfhi(kin.ci << msh)}; PIN(kg.airev); pinc(kg.C);
    }
    if (k == 3) { kg.mx = dot2bf(kin.ci & 0x7fff7fffu, 0x3f803f80u, dot2bf(kin.cr & 0x7fff7fffu, 0x3f803f80u, 0.f)); kg.sm = dot2bf(kin.cr, 0x3f803f80u, 0.f); PIN2(kg.mx, kg.sm); }
    if (k == 4) { kg.sm = fold32(kg.sm, kg.mx); kg.sm = fold16(kg.sm, kg.sm); }
    if (k == 5) { kg.sm += dppf<0x128>(kg.sm); kg.sm += dppf<0x124>(kg.sm); }
    if (k == 6) { kg.sm += dppf<0x122>(kg.sm); kg.sm += dppf<0x121>(kg.sm); kg.sm *= 2.f * kg.dt; }
    if (k >= 4 && k <= 6) PIN(kg.sm);
    if (k == 7) {
      kg.dsk = bfhi((u32)__builtin_amdgcn_readlane((int)kin.misc, 1) << csh) + rdl(kg.sm, 0);
      const u32 se = 268u - ((rdu(kg.sm, 32) >> 23) & 0xff);
      kg.mx = asf((se > 254u ? 254u : se) << 23); PIN(kg.dsk);
    }
    if (k == 8) {
      kg.inv = asf((254u << 23) - asu(kg.mx));
      const float cs = 2.f * kg.dt * kg.mx;
      kg.C.r *= cs; kg.C.i *= cs; PIN(kg.inv); pinc(kg.C);
    }
    if (k == 9) { kg.qa[1] = cexp_at(kg.ar2, kg.airev, 128.f); pinc(kg.qa[1]); }
    if (k == 10) { kg.qb[1] = csqr(kg.qa[1]); pinc(kg.qb[1]); }
    if (k == 11) { kg.w512 = csqr(kg.qb[1]); kg.qb[0] = sel(kg.qa[1], kg.w512); pinc(kg.qb[0]); }
    if (k == 12) { kg.s1 = cexp_at(kg.ar2, kg.airev, th ? 1.f : 8.f); kg.s1.i = th ? kg.s1.i : -kg.s1.i; pinc(kg.s1); }
    if (k == 13) { kg.m[0] = csqr(kg.s1); pinc(kg.m[0]); }
    if (k == 14) { kg.m[1] = csqr(kg.m[0]); kg.qa[0] = sel(kg.m[1], kg.qb[1]); pinc(kg.m[1]); pinc(kg.qa[0]); }
    if (k == 15) { kg.m[2] = sel(kg.qa[1], csqr(kg.m[1])); pinc(kg.m[2]); }
    if (k == 16) { kg.v[0] = sel(cmul(kg.C, kg.s1), cpx{1.f, 0.f}); pinc(kg.v[0]); }
    if (k == 17) { kg.v[1] = cmul(kg.v[0], kg.s1); pinc(kg.v[1]); }
    if (k == 18) { kg.v[16] = cexp_at(kg.ar2, kg.airev, th ? 258.f : 1032.f); pinc(kg.v[16]); }
    if (k == 19) { kg.v[16] = cmul(kg.v[16], kg.C); pinc(kg.v[16]); }
    if (k == 20) { kg.v[17] = cmul(kg.v[16], sel(kg.m[0], kg.qa[1])); pinc(kg.v[17]); }
    if (k == 21) { kg.v[24] = cmul(kg.v[17], kg.qb[1]); kg.v[25] = cmul(kg.v[24], kg.m[1]); pinc(kg.v[24]); pinc(kg.v[25]); }
  };
  u32 kre[16], kim[16];
  auto kx_mul = [&](int d, int s, cpx m) AI { kg.v[d] = cmul(kg.v[s], m); PIN2(kg.v[d].r, kg.v[d].i); };
  auto kx_pack = [&](int p) AI { kre[p] = pk_f16(kg.v[2 * p].r, kg.v[2 * p + 1].r); kim[p] = pk_f16(kg.v[2 * p].i, kg.v[2 * p + 1].i); PIN2(kre[p], kim[p]); };
  auto kx_piece = [&](int k) AI {
    if (k < 20) {
      const int q = k / 10, j = k % 10, o = 16 + 8 * q;
      if (j < 2) kx_mul(o + 2 + j, o + j, kg.qa[q]);
      else if (j < 6) kx_mul(o + 2 + j, o + j - 2, kg.qb[q]);
      else kx_pack(o / 2 + j - 6);
    } else {
      const int j = k - 20;
      if (j < 2) kx_mul(2 + j, j, kg.m[0]);
      else if (j < 6) kx_mul(2 + j, j - 2, kg.m[1]);
      else {
        const int p = (j - 6) >> 2, r = (j - 6) & 3;
        if (r < 2) kx_mul(8 + 2 * p + r, 2 * p + r, kg.m[2]);
        else kx_pack(p + 4 * (r - 2));
      }
    }
  };
  auto kt_store = [&](int i) AI {
    const int c = i >> 1;
    u32 a = SCR + ch * 2 * KG_PLANE + (64 * ((w >> 1) & 1) + lane) * KG_ROW + (th ? c * 16 : (c < 2 ? 80 + c * 16 : 64)) + (i & 1) * KG_PLANE;
    if (c == 3) a = th ? a : LDS_KD + 4096 + lane * 16;
    const u32* p = (i & 1) ? kim : kre;
    lds_st<u32x4>(a, u32x4{p[4 * c], p[4 * c + 1], p[4 * c + 2], p[4 * c + 3]});
  };
  const int kt = v4 < 3 ? v4 : 0;
  const u32 krb = SCR + ch * 2 * KG_PLANE + (8 * lg + lq) * KG_ROW + lp * 8, kra = krb + kt * 32;
  u32x4 kA[8], kB[8];
  f32x4 ka = {0.f, 0.f, 0.f, 0.f}, kb = ka;
  auto kt_rd = [&](int k) AI {
    const u32 o = k * 32 * KG_ROW;
    kA[k] = cat(lds_tr(kra + o), lds_tr(kra + o + 4 * KG_ROW)); kB[k] = cat(lds_tr(krb + 80 + o), lds_tr(krb + 80 + o + 4 * KG_ROW));
  };
  auto kt_mma = [&](int k) AI { if (k & 1) kb = mma_h(kB[k], kA[k], kb); else ka = mma_h(kB[k], kA[k], ka); };
  auto kt_put = [&]() AI {
    ka += kb;
    lds_st<f32x2>(LDS_SC + ch * 8, f32x2{kg.inv, kg.dsk});
    lds_st<u32x2>(LDS_KD + ch * 1536 + ((16 * kt + lc) * 16 + 4 * lg) * 2, u32x2{pk_f16(ka[0], ka[1]), pk_f16(ka[2], ka[3])});
  };
  u32x4 uv, kdv; u32 unx, kgv[8];
  const int g8 = tid & 255;
  auto load_u = [&]() AI {
    const u32 cb = (2 * b + ch) * 128, gn = g8 + 1;
    uv = ld16_async(rxt, (g8 >> 3) * XT_BLK + (g8 & 7) * 16 + cb, 0);
    asm volatile("buffer_load_dword %0, %1, %2, 0 offen sc1" : "=v"(unx) : "v"((gn >> 3) * XT_BLK + (gn & 7) * 16 + cb), "s"(rxt) : "memory");
    asm volatile("global_load_dwordx4 %0, %1, %2" : "=v"(kdv) : "v"(g8 * 16), "s"(&kd_tab) : "memory");
  };
  auto kt_fin = [&](int k) AI {
    if (k == 0) { PIN(uv); PIN(unx); PIN(kdv); }
    if (k < 4) { kgv[2 * k] = lds_ld<u16>(LDS_KD + ch * 1536 + (kdv[k] & 0xffffu)); kgv[2 * k + 1] = lds_ld<u16>(LDS_KD + ch * 1536 + (kdv[k] >> 16)); }
    if (k == 4) lds_st<u32x4>(LDS_U + ch * U_CH + 32 + g8 * 16, uv);
    if (k == 5) lds_st<u32x4>(LDS_U + ch * U_CH + U_COPY + 32 + g8 * 16,
                              u32x4{__builtin_amdgcn_alignbit(uv[1], uv[0], 16), __builtin_amdgcn_alignbit(uv[2], uv[1], 16),
                                    __builtin_amdgcn_alignbit(uv[3], uv[2], 16), __builtin_amdgcn_alignbit(unx, uv[3], 16)});
    if (k == 6) {
      u32 za = LDS_U + ch * U_CH + ((g8 + 3) & 1) * 16 + (((g8 + 3) >> 1) & 1) * U_COPY, zb = LDS_KX + ch * KX_STRIDE + (g8 & 31) * 16;
      PIN2(za, zb);
      lds_st<u32x4>(g8 < 4 ? za : zb, u32x4{0, 0, 0, g8 == 0 ? uv[0] << 16 : 0u});
    }
    if (k == 7) {
#pragma unroll
      for (int j = 0; j < 8; ++j) PIN(kgv[j]);
      lds_st<u32x4>(LDS_KX + ch * KX_STRIDE + 512 + g8 * 16, u32x4{kgv[0] | (kgv[1] << 16), kgv[2] | (kgv[3] << 16), kgv[4] | (kgv[5] << 16), kgv[6] | (g8 == 0 ? 0u : kgv[7] << 16)});
    }
  };
  const char* pfx = P.ws + WS_FX + (4 * w + lp) * FS;

  f32x16 ag[2], au[2];
#pragma unroll
  for (int i = 0; i < 16; ++i) { ag[0][i] = 0.f; ag[1][i] = 0.f; au[0][i] = 0.f; au[1][i] = 0.f; }
  float eke[2] = {1.f, 1.f}, es2[2] = {1.f, 1.f};
  rsrc_t rhs = mk_rsrc(P.ws + WS_H, is_aux ? 0 : H_BYTES);
  const u32 ho = (u32)(((mb * 4 + th * 2) * 112 + nb * 8 + fq * 2) * 1024 + lane * 16);
  float ee[4], eg[4]; u32x4 eo;
  auto epi_piece = [&](int t2, int k) AI {
    const int q = k / 5, j = k % 5;
#pragma unroll
    for (int i = 0; i < 4; ++i) {
      if (j == 0) { ee[i] = ag[t2][4 * q + i] * eke[t2]; eg[i] = ag[t2][4 * q + i] * au[t2][4 * q + i]; }
      if (j == 1) ee[i] = __builtin_amdgcn_exp2f(ee[i]);
      if (j == 2) ee[i] = FMA(ee[i], es2[t2], es2[t2]);
      if (j == 3) ee[i] = __builtin_amdgcn_rcpf(ee[i]);
      if (j == 4) eg[i] *= ee[i];
    }
    if (j < 4) asm volatile("" : "+v"(ee[0]), "+v"(ee[1]), "+v"(ee[2]), "+v"(ee[3]), "+v"(eg[0]), "+v"(eg[1]), "+v"(eg[2]), "+v"(eg[3]));
    if (j == 4) {
      eo[2 * (q & 1)] = pk_bf16(eg[0], eg[1]); eo[2 * (q & 1) + 1] = pk_bf16(eg[2], eg[3]);
      if (q & 1) __builtin_amdgcn_raw_buffer_store_b128(eo, rhs, ho + (t2 * 112 + (q >> 1)) * 1024, 0, 16);
      else PIN2(eo[0], eo[1]);
    }
  };

  if (__builtin_expect(!is_aux, 1)) {
#pragma unroll
    for (int i = 0; i < 12; ++i) { if (i == 0) STAMP(12); g1_piece(i / 6, i % 6); if (i == 5) { STAMP(13); BAR(); } }
    STAMP(2); CSTAMP(59);
    u32 xa[4], wa;
    {
      const int row = 64 * th + (lane & 31);
#pragma unroll
      for (int ks = 0; ks < 4; ++ks) xa[ks] = SLOT + row * 128 + (((2 * ks + lh) ^ ((row >> 1) & 7)) * 16);
      wa = SLOT + 16384 + (8 * lh + lq) * 256 + ((fq ^ lq) * 64) + (4 * (lg & 1) + ((lp & 1) << 1) + (lp >> 1)) * 8;
    }
    const int G = (lane & 31) >> 2;
    const char* pcs = P.ws + WS_CS + (size_t)(lh * F + nb * 128 + fq * 32 + (4 * (G >> 2) + 2 * (G & 1) + ((G >> 1) & 1)) * 4 + lp) * 8;
    const char* pst = P.ws + WS_ST + (size_t)(mb * 128 + 64 * th + lane) * 8;
    u64 gc, gs, fx; u32 gst[2];
    u32x4 xg, xu, xm[2];
    WAIT_V(6);
    STAMP(15);
    BAR();
    STAMP(3);
    u32x4 fx0[4], fx1[4], fg[4], fu[4];
    auto rd0 = [&](int d, int ks) AI { fx0[d] = lds_ld<u32x4>(xa[ks]); fg[d] = cat(lds_tr(wa + ks * 4096), lds_tr(wa + ks * 4096 + 1024)); };
    auto rd1 = [&](int d, int ks) AI { fu[d] = cat(lds_tr(wa + 16384 + ks * 4096), lds_tr(wa + 16384 + ks * 4096 + 1024)); fx1[d] = lds_ld<u32x4>(xa[ks] + 4096); };
    rd0(0, 0); rd1(0, 0);
    g1_piece(2, 0); g1_piece(2, 1);
    auto filler = [&](int n) AI {
      if (n >= 9 && n < 12) small_loads(n - 9);
      if (n == 28) { tag = mk_tag(kin.misc); t32 = (u32)tag; }
      if (n >= 29 && n < 58 && n % 4 != 0) seed_piece(n - 29 - (n - 29) / 4);
      if (n >= 58 && n < 110 && n % 5 != 2) kx_piece(n - 58 - (n - 58) / 5);
      if (n == 92) fx = ld8_async(pfx);
      if (n == 93) { gc = ld8_async(pcs); gs = ld8_async(pst); }
      if (n == 120) {
        WAIT_V(10);
        PIN(gc); PIN(gs); PIN(fx);
        auto ok = [&]() AI { return all_of((((u32)(gc >> 32) ^ t32) | ((u32)(gs >> 32) ^ t32) | ((u32)fx ^ t32) | ((u32)(fx >> 32) ^ (u32)(tag >> 32))) == 0); };
        if (__builtin_expect(!ok(), 0)) {
#pragma unroll 1
          for (u32 sp = 0; sp < SPIN_CAP; ++sp) {
            gc = ld_agent(pcs); gs = ld_agent(pst); fx = ld_agent(pfx);
            if (ok()) break;
            __builtin_amdgcn_s_sleep(1);
          }
        }
      }
      if (n == 121) {
        const float cv = asf((u32)gc);
        const float c0 = rbf(cv), r1 = cv - c0, c1 = rbf(r1);
        const u32 e0 = pk_bf16(c0, c1), e1 = pk_bf16(r1 - c1, 0.f);
        xg = u32x4{lh ? 0u : e0, lh ? 0u : e1, 0, 0}; xu = u32x4{lh ? e0 : 0u, lh ? e1 : 0u, 0, 0};
        PIN2(xg, xu);
        auto r = __builtin_amdgcn_permlane32_swap((u32)gs, (u32)gs, false, false);
        gst[0] = r[0]; gst[1] = r[1];
      }
      if (n == 122 || n == 123) {
        const int t = n - 122;
        const u32 m = (gst[t] & 0xffffu) ^ 0x8000u;
        xm[t] = u32x4{m | (m << 16), m, 0, 0};
        const float sd = bfhi(gst[t]);
        eke[t] = -1.44269504088896f * __builtin_amdgcn_rcpf(sd); es2[t] = sd * sd;
        PIN(xm[t]); PIN2(eke[t], es2[t]);
      }
    };
    sfor<0, 28>([&](auto I) AI {
      constexpr int s = I / 4, ks = I % 4;
      if (ks == 3) {
        STAMP(16 + 2 * s);
        if (s == 0) WAIT_V(11); else if (s == 6) WAIT_V(9); else if (s < 7) WAIT_V(6);
        BAR();
        if (s < 7) {
          const int dl = (s + 2) % 3 == 0 ? -2 * SLOT : SLOT;
#pragma unroll
          for (int i = 0; i < 4; ++i) { xa[i] += dl; PIN(xa[i]); }
          wa += dl; PIN(wa);
        }
        STAMP(17 + 2 * s);
      }
      const int n = s * 16 + ks * 4, kn = (ks + 1) & 3;
      const int rs = ks == 3 ? s : s - 1, pi = ks == 3 ? 0 : 2 * ks + 2;
      auto piece = [&](int i) AI {
        if (i > 5) return;
        if (rs + 3 < 8) g1_piece(rs + 3, i);
        else if (rs == 5) w2_piece(i, 0);
        else if (rs == 6) w2_piece(6 + i, SLOT);
      };
      SCHED(); ag[0] = mma32(fg[0], fx0[0], ag[0]); SCHED();
      rd0(1, kn);
      filler(n);
      SCHED(); au[0] = mma32(fu[0], fx0[0], au[0]); SCHED();
      rd1(1, kn);
      filler(n + 1);
      SCHED(); ag[1] = mma32(fg[0], fx1[0], ag[1]); SCHED();
      filler(n + 2); piece(pi);
      SCHED(); au[1] = mma32(fu[0], fx1[0], au[1]); SCHED();
      filler(n + 3); piece(pi + 1);
      SCHED();
      fx0[0] = fx0[1]; fx1[0] = fx1[1]; fg[0] = fg[1]; fu[0] = fu[1];
    });
    sfor<0, 4>([&](auto I) AI {
      constexpr int ks = I;
      SCHED(); ag[0] = mma32(fg[ks], fx0[ks], ag[0]); SCHED();
      if (ks < 3) rd0(ks + 1, ks + 1);
      if (ks == 0) { w2_piece(8, SLOT); w2_piece(9, SLOT); }
      if (ks == 1) filler(120);
      if (ks == 2) filler(122);
      if (ks == 3) { STAMP(30); WAIT_V(6); BAR(); STAMP(31); load_u(); }
      SCHED(); au[0] = mma32(fu[ks], fx0[ks], au[0]); SCHED();
      if (ks < 3) rd1(ks + 1, ks + 1);
      if (ks == 1) { filler(121); w2_piece(10, SLOT); w2_piece(11, SLOT); }
      if (ks == 2) filler(123);
    });
    SCHED(); ag[0] = mma32(xg, xm[0], ag[0]); SCHED(); kt_store(0);
    SCHED(); au[0] = mma32(xu, xm[0], au[0]); SCHED(); kt_store(1);
    sfor<0, 10>([&](auto I) AI {
      constexpr int i = I;
      SCHED();
      if (i < 8) { if (i & 1) au[1] = mma32(fu[i >> 1], fx1[i >> 1], au[1]); else ag[1] = mma32(fg[i >> 1], fx1[i >> 1], ag[1]); }
      else if (i == 8) ag[1] = mma32(xg, xm[1], ag[1]); else au[1] = mma32(xu, xm[1], au[1]);
      SCHED();
      if (i < 6) kt_store(2 + i);
      if (i > 0) { epi_piece(0, 2 * i - 2); epi_piece(0, 2 * i - 1); }
    });
    epi_piece(0, 18); epi_piece(0, 19);
    STAMP(4);
  } else {
    const int a = xcd * 4 + idx - 28, ca = a < 28 ? a : 27;
    const int cmat = ca / 14, cnb = ca % 14;
    rsrc_t rc = mk_rsrc(cmat == 1 ? P.w3 : P.w1, D * F * 2);
    const u32 vo = (tid >> 4) * (F * 2) + cnb * 256 + lc * 16;
    u32x4 wv[16], xv[8];
    float s1[8], s2[8];
#pragma unroll
    for (int i = 0; i < 8; ++i) xv[i] = __builtin_amdgcn_raw_buffer_load_b128(rx, lane * 16, (64 * a + 8 * w + i) * 1024, 0);
    small_loads(0); small_loads(1); small_loads(2);
    WAIT_V(4);
    tag = mk_tag(kin.misc); t32 = (u32)tag;
#pragma unroll
    for (int i = 0; i < 8; ++i) {
      s1[i] = 0.f; s2[i] = 0.f;
#pragma unroll
      for (int j = 0; j < 4; ++j) { s1[i] = dot2bf(xv[i][j], 0x3f803f80u, s1[i]); s2[i] = dot2bf(xv[i][j], xv[i][j], s2[i]); }
      wv[i] = __builtin_amdgcn_raw_buffer_load_b128(rc, vo, i * 32 * F * 2, 0);
      SCHED();
    }
    float rs[2], mr[2];
#pragma unroll
    for (int g = 0; g < 2; ++g) {
      const float S = row_sum(fold16(fold32(s1[4 * g], s1[4 * g + 2]), fold32(s1[4 * g + 1], s1[4 * g + 3])));
      const float Q = row_sum(fold16(fold32(s2[4 * g], s2[4 * g + 2]), fold32(s2[4 * g + 1], s2[4 * g + 3])));
      const float mf = S * (1.f / D);
      const float var = rbf(__builtin_fmaxf(Q * (1.f / D) - mf * mf, 0.f));
      const float m = rbf(mf);
      const float sd = rbf(__builtin_amdgcn_sqrtf(rbf(var + 1e-5f)));
      rs[g] = __builtin_amdgcn_rcpf(sd);
      mr[g] = -m * rs[g];
      __builtin_amdgcn_raw_buffer_store_b64(u32x2{asu(sd) | (asu(m) >> 16), t32}, mk_rsrc(P.ws + WS_ST, L * 8), lc == 0 ? (u32)(64 * a + 8 * w + 4 * g + lg) * 8 : OOB, 0, 16);
    }
    float rr[8], cc[8];
#pragma unroll
    for (int i = 0; i < 8; ++i) { rr[i] = rdl(rs[i >> 2], (i & 3) * 16); cc[i] = rdl(mr[i >> 2], (i & 3) * 16); }
#pragma unroll
    for (int e = 0; e < 8; ++e) {
      u32x4 o;
#pragma unroll
      for (int j = 0; j < 4; ++j) {
        const u32 v0 = xv[2 * j][e >> 1], v1 = xv[2 * j + 1][e >> 1];
        o[j] = pk_f16(FMA((e & 1) ? bfhi(v0) : bf2f(v0), rr[2 * j], cc[2 * j]), FMA((e & 1) ? bfhi(v1) : bf2f(v1), rr[2 * j + 1], cc[2 * j + 1]));
      }
      lds_st<u32x4>((u32)((8 * lane + e) * 128 + ((w ^ (lane & 7)) * 16)), o);
      wv[8 + e] = __builtin_amdgcn_raw_buffer_load_b128(rc, vo, (8 + e) * 32 * F * 2, 0);
      SCHED();
    }
    BAR();
#pragma unroll
    for (int j = 0; j < 8; ++j) {
      const int d = (tid >> 3) + 64 * j, sl = tid & 7;
      __builtin_amdgcn_raw_buffer_store_b128(lds_ld<u32x4>((u32)(d * 128 + ((sl ^ ((d >> 3) & 7)) * 16))), rxt, a * XT_BLK + d * 128 + sl * 16, 0, 16);
    }
    u32x4 sel;
    const u32 one = (lc & 1) ? 0x3f800000u : 0x3f80u;
    sel[0] = (lc >> 1) == 0 ? one : 0; sel[1] = (lc >> 1) == 1 ? one : 0;
    sel[2] = (lc >> 1) == 2 ? one : 0; sel[3] = (lc >> 1) == 3 ? one : 0;
    f32x4 c0 = {0.f, 0.f, 0.f, 0.f}, c1 = c0;
#pragma unroll
    for (int k = 0; k < 16; k += 2) { c0 = mma_bf(sel, wv[k], c0); c1 = mma_bf(sel, wv[k + 1], c1); }
    if (lane < 32) lds_st<f32x4>(65536 + w * 512 + lc * 32 + lg * 16, c0 + c1);
    WAIT_V(0);
    BAR();
    if (tid == 0) st_agent(P.ws + WS_FX + a * FS, tag);
    if (tid < 128) {
      float sum = 0.f;
#pragma unroll
      for (int j = 0; j < 8; ++j) sum += lds_ld<float>(65536 + j * 512 + tid * 4);
      st_agent(P.ws + WS_CS + (size_t)(cmat * F + cnb * 128 + tid) * 8, ((u64)t32 << 32) | asu(sum));
    }
    STAMP(2); CSTAMP(59);
#pragma unroll
    for (int k = 0; k < 22; ++k) seed_piece(k);
#pragma unroll
    for (int k = 0; k < 42; ++k) kx_piece(k);
#pragma unroll
    for (int i = 0; i < 8; ++i) kt_store(i);
    STAMP(17);
    wait_flags(pfx, tag);
    BAR();
    load_u();
#pragma unroll
    for (int i = 0; i < 12; ++i) w2_piece(i, (i / 6) * SLOT);
    WAIT_V(0);
  }
  STAMP(5); CSTAMP(60);
  BAR();
  STAMP(32);
  u32 w2a[2];
  w2a[0] = v4 * 12288 + (8 * lh + lq) * 128 + (4 * ((lg ^ v4) & 1) + ((lp & 1) << 1) + (lp >> 1)) * 8 + (((lq >> 1) ^ (v4 >> 1)) & 1) * 64;
  w2a[1] = w2a[0] ^ 64;
  u32x4 wr[28][2];
  auto w2_rd = [&](int i) AI { const int k = i >> 1; const u32 a = w2a[i & 1] + (k < 24 ? ((k / 6) & 1) * SLOT : 0) + (k % 6) * 2048; wr[k][i & 1] = cat(lds_tr(a), lds_tr(a + 512)); };
  sfor<0, 20>([&](auto I) AI {
    constexpr int k = I;
    SCHED();
    if (k < 8) kt_rd(k); else w2_rd(k - 8);
    if (k >= 3 && k < 11) kt_mma(k - 3);
    if (k == 14) kt_put();
    SCHED();
    epi_piece(1, k);
  });
  STAMP(34);
  WAIT_V(4);
  BAR();
  STAMP(35);
  sfor<0, 8>([&](auto I) AI { kt_fin(I); });
  STAMP(39);
  WAIT_V(0);
  STAMP(40);
  __builtin_amdgcn_raw_buffer_store_b64(u32x2{t32, (u32)(tag >> 32)}, mk_rsrc(P.ws + WS_FH, is_aux ? 0 : 2048 * FS), lane == 0 ? (u32)(((mb * 2 + th) * 64 + nb * 4 + fq) * FS) : OOB, 0, 16);
  BAR();
  STAMP(6);
  rsrc_t rh = mk_rsrc(P.ws + WS_H, H_BYTES);
  const u32 hso = (u32)(((tt * 2 + ch) * 112 + v4 * 28) * 1024);
  u32x4 hf[28];
  auto load_h = [&](int i) AI { hf[i] = ld16_async(rh, lane * 16, hso + i * 1024); };
  const int trow = tt * 64 + ch * 32 + (lane & 31), dcol = nb2 * 64 + 16 * v4 + 8 * lh;
  u32x4 xres;
  u32x2 uu[2];
  f32x4 csc;
  {
    f32x4 ya[8];
#pragma unroll
    for (int T = 0; T < 8; ++T) ya[T] = f32x4{0.f, 0.f, 0.f, 0.f};
    const int s00 = 16 * (lg >> 1) + lc - 8 * (lg & 1) - 7;
    const int par = s00 & 1;
    const u32 abase = LDS_U + ch * U_CH + par * U_COPY + 32 + (s00 - par) * 2 + v4 * 128;
    const u32 bbase = LDS_KX + ch * KX_STRIDE + 512 + (16 * (lc - (lg >> 1)) + 8 * (lg & 1)) * 2 - v4 * 128 - 64;
    auto rdA = [&](int ph, int mu) AI {
      const u32 a = abase + ph * 64 + mu * 512;
      return u32x4{lds_ld<u32>(a), lds_ld<u32>(a + 4), lds_ld<u32>(a + 8), lds_ld<u32>(a + 12)};
    };
    const char* pfh = P.ws + WS_FH + (tt * 64 + 14 * v4 + (lane < 14 ? lane : 0)) * FS;
    u64 fh = 0;
    u32x4 B[8], A[3];
    csc = lds_ld<f32x4>(LDS_SC);
    auto put_y = [&](int T) AI { lds_st<f32x4>(SCR + ((T ^ 4) * 8 + w) * 1024 + lane * 16, ya[T]); };
    A[0] = rdA(0, 0);
#pragma unroll
    for (int g = 7; g >= 0; --g) B[g] = lds_ld<u32x4>(bbase + 64 + g * 512);
    A[1] = rdA(0, 1);
    sfor<0, 16>([&](auto I) AI {
      constexpr int st = I, ph = st >> 3, mu = st & 7;
      SCHED();
      if (st + 2 < 16) A[(st + 2) % 3] = rdA((st + 2) >> 3, (st + 2) & 7);
      sfor<0, 8 - mu>([&](auto J) AI {
        constexpr int T = 7 - J, c = 36 * ph + mu * 8 - mu * (mu - 1) / 2 + J;
        SCHED();
        ya[T] = mma_h(A[st % 3], B[T - mu], ya[T]);
        SCHED();
        if (T == 7 && ph == 0) B[7 - mu] = lds_ld<u32x4>(bbase + (7 - mu) * 512);
        if (c < 24 && (c & 1)) w2_rd(12 + (c >> 1));
        if (c < 24 && c % 4 == 0) w2_piece(12 + c / 4, 0);
        if (c == 29) BAR();
        if (c >= 30 && c < 42 && !(c & 1)) w2_piece(18 + (c - 30) / 2, SLOT);
        if (c == 25) asm volatile("buffer_load_dwordx4 %0, %1, %2, 0 offen" : "=v"(xres) : "v"((u32)((trow * D + dcol) * 2)), "s"(rx) : "memory");
        if (c == POLLC) fh = ld8_async(pfh);
        if (c == CHKC) {
          STAMP(41);
          WAIT_V(0);
          PIN(fh);
          if (b == 255 && w == 7) {
            const char* p = P.ws + WS_FH + (lg * 128 + (lc < 14 ? lc : 0) * 4) * FS;
#pragma unroll 1
            for (u32 sp = 0; sp < SPIN_CAP; ++sp) {
              const u64 v0 = ld_agent(p), v1 = ld_agent(p + 512 * FS), v2 = ld_agent(p + 1024 * FS), v3 = ld_agent(p + 1536 * FS);
              if (all_of(((v0 ^ tag) | (v1 ^ tag) | (v2 ^ tag) | (v3 ^ tag)) == 0)) break;
              __builtin_amdgcn_s_sleep(1);
            }
            if (lane == 0) st_agent(P.ws + WS_EP, tag);
          }
          if (!all_of(fh == tag)) wait_flags(pfh, tag);
          STAMP(8);
        }
        if (c >= CHKC && c - CHKC < HW) load_h(c - CHKC);
      });
      SCHED();
      if (st == 12) {
#pragma unroll
        for (int i = 0; i < 2; ++i) uu[i] = lds_ld<u32x2>(LDS_U + i * U_CH + 32 + (256 * w + 16 * lc + 4 * lg) * 2);
      }
      if (st == 13) BAR();
      if (st >= 9) put_y(mu - 1);
    });
    STAMP(7);
    put_y(7);
#pragma unroll
    for (int i = 72 - CHKC; i < HW; ++i) load_h(i);
    BAR();
  }
  STAMP(9); CSTAMP(61);
  {
    f32x16 oa[2];
#pragma unroll
    for (int i = 0; i < 16; ++i) { oa[0][i] = 0.f; oa[1][i] = 0.f; }
    rsrc_t ra = mk_rsrc(P.ws + WS_AO, AO_BYTES);
    u32x2 ao[4];
    const u32 aoff = (u32)((dcol >> 1) * AO_ROW + (trow >> 8) * 2048 + ((trow >> 1) & 1) * 1024 + ((trow >> 2) & 63) * 16 + (trow & 1) * 8);
    f32x4 y[8];
    sfor<0, 24>([&](auto I) AI {
      constexpr int s = I;
      if (HW + s < 28) load_h(HW + s);
      asm volatile("s_waitcnt vmcnt(%1)" : "+v"(hf[s]) : "n"(g2_wait_hf(s)) : "memory");
      if (s == 0) STAMP(46);
      if (s == 5) { STAMP(48); BAR(); STAMP(49); }
      if (s == 24 - PD) { STAMP(50); WAIT_V(G2_E); BAR(); w2a[0] += 2 * SLOT; w2a[1] += 2 * SLOT; PIN2(w2a[0], w2a[1]); STAMP(51); }
#pragma unroll
      for (int i = 0; i < 2; ++i) {
        SCHED();
        oa[i] = mma32(wr[s][i], hf[s], oa[i]);
        SCHED();
        if (s < 2) {
          y[4 * s + 2 * i] = lds_ld<f32x4>(SCR + ((w ^ 4) * 8 + 4 * s + 2 * i) * 1024 + lane * 16);
          y[4 * s + 2 * i + 1] = lds_ld<f32x4>(SCR + ((w ^ 4) * 8 + 4 * s + 2 * i + 1) * 1024 + lane * 16);
        }
        if (s == 2) {
          y[i] = (y[4 * i] + y[4 * i + 1]) + (y[4 * i + 2] + y[4 * i + 3]);
#pragma unroll
          for (int r = 0; r < 4; ++r) y[i][r] = FMA(y[i][r], csc[2 * i], h2f((uu[i][r >> 1] >> (16 * (r & 1))) & 0xffff) * csc[2 * i + 1]);
          PIN(y[i]);
        }
        if (s == 3)
          __builtin_amdgcn_raw_buffer_store_b128(u32x4{pk_bf16(y[0][2 * i], y[1][2 * i]), t32, pk_bf16(y[0][2 * i + 1], y[1][2 * i + 1]), t32}, ra,
                                                 b * AO_ROW + w * 2048 + (lc * 4 + lg) * 16 + i * 1024, 0, 16);
        if (s >= 5 && s <= 8 && i == 1) w2_piece(24 + s - 5, 2 * SLOT);
        if (s >= 20 && s <= 23 && i == 0)
          asm volatile("buffer_load_dwordx2 %0, %1, %2, %3 offen sc1" : "=v"(ao[s - 20]) : "v"(aoff), "s"(ra), "s"((s - 20) * AO_ROW) : "memory");
        if (s + PD >= 12 && s + PD < 28) w2_rd(2 * (s + PD) + i);
      }
    });
    auto put_o = [&](int j) AI {
      const int g = 1 + (j >> 1), o = (g & 1) * 8 + (j & 1) * 4;
      const f32x16& A = oa[g >> 1];
      lds_st<f32x4>(v4 * 12288 + ch * 6144 + lane * 16 + j * 1024, f32x4{A[o], A[o + 1], A[o + 2], A[o + 3]});
    };
    asm volatile("s_waitcnt vmcnt(4)" : "+v"(hf[24]), "+v"(hf[25]), "+v"(hf[26]), "+v"(hf[27]) :: "memory");
    sfor<24, 28>([&](auto I) AI { oa[1] = mma32(wr[I][1], hf[I], oa[1]); });
    sfor<24, 28>([&](auto I) AI { SCHED(); oa[0] = mma32(wr[I][0], hf[I], oa[0]); SCHED(); if (I > 24) put_o(I - 23); });
    put_o(5);
    STAMP(10);
    put_o(0); put_o(1);
    WAIT_V(0);
    PIN(xres);
#pragma unroll
    for (int e = 0; e < 4; ++e) PIN(ao[e]);
    auto ok = [&]() AI { return all_of(((ao[0][1] ^ t32) | (ao[1][1] ^ t32) | (ao[2][1] ^ t32) | (ao[3][1] ^ t32)) == 0); };
    if (__builtin_expect(!ok(), 0)) {
#pragma unroll 1
      for (u32 sp = 0; sp < SPIN_CAP; ++sp) {
#pragma unroll
        for (int e = 0; e < 4; ++e) ao[e] = __builtin_amdgcn_raw_buffer_load_b64(ra, aoff, e * AO_ROW, 16);
        if (ok()) break;
        __builtin_amdgcn_s_sleep(1);
        asm volatile("" ::: "memory");
      }
    }
    STAMP(52);
    float r[8];
#pragma unroll
    for (int e = 0; e < 4; ++e) {
      r[2 * e] = oa[0][2 * e] + (bf2f(xres[e]) + bf2f(ao[e][0])); r[2 * e + 1] = oa[0][2 * e + 1] + (bfhi(xres[e]) + bfhi(ao[e][0]));
      PIN2(r[2 * e], r[2 * e + 1]);
    }
    BAR();
    f32x4 p[6];
#pragma unroll
    for (int g = 1; g < 4; ++g) {
      const u32 a = (v4 ^ g) * 12288 + ch * 6144 + lane * 16 + (g - 1) * 2048;
      p[2 * g - 2] = lds_ld<f32x4>(a); p[2 * g - 1] = lds_ld<f32x4>(a + 1024);
    }
    SCHED();
#pragma unroll
    for (int g = 0; g < 3; ++g)
#pragma unroll
      for (int e = 0; e < 4; ++e) { r[e] += p[2 * g][e]; r[4 + e] += p[2 * g + 1][e]; }
    __builtin_amdgcn_raw_buffer_store_b128(u32x4{pk_bf16(r[0], r[1]), pk_bf16(r[2], r[3]), pk_bf16(r[4], r[5]), pk_bf16(r[6], r[7])}, mk_rsrc(P.out, L * D * 2), (trow * D + dcol) * 2, 0, 0);
    STAMP(11); CSTAMP(62);
    STAMP_OUT;
  }
}

#if defined(__HIP_DEVICE_COMPILE__) && !PKEPI
#pragma clang attribute pop
#endif
extern "C" void kernel_launch(void* const* d_in, const int* in_sizes, int n_in,
                              void* d_out, int out_size, void* d_ws, size_t ws_size,
                              hipStream_t stream) {
  Params P;
  P.x = (const u16*)d_in[0]; P.lar = (const u16*)d_in[1]; P.aim = (const u16*)d_in[2];
  P.cre = (const u16*)d_in[3]; P.cim = (const u16*)d_in[4]; P.ldt = (const u16*)d_in[5];
  P.dsk = (const u16*)d_in[6]; P.w1 = (const u16*)d_in[8]; P.w3 = (const u16*)d_in[9];
  P.w2 = (const u16*)d_in[10]; P.out = (u16*)d_out; P.ws = (char*)d_ws;
  (void)hipFuncSetAttribute((const void*)fused, hipFuncAttributeMaxDynamicSharedMemorySize, LDS_TOTAL);
  fused<<<256, 512, LDS_TOTAL, stream>>>(P);
}
```

```cpp
#include <hip/hip_runtime.h>
#include <stdint.h>
#define PKEPI 0
#if defined(__HIP_DEVICE_COMPILE__) && !PKEPI
#pragma clang attribute push (__attribute__((target("no-packed-fp32-ops"))), apply_to = function)
#endif

typedef unsigned short u16;
typedef unsigned int u32;
typedef unsigned long long u64;
typedef __attribute__((ext_vector_type(4))) short s16x4;
typedef __attribute__((ext_vector_type(8))) __bf16 bf16x8;
typedef __attribute__((ext_vector_type(8))) _Float16 f16x8;
typedef __attribute__((ext_vector_type(2))) __bf16 bf16x2;
typedef __attribute__((ext_vector_type(2))) _Float16 f16x2;
typedef __attribute__((ext_vector_type(16))) float f32x16;
typedef __attribute__((ext_vector_type(4))) float f32x4;
typedef __attribute__((ext_vector_type(2))) float f32x2;
typedef __attribute__((ext_vector_type(4))) u32 u32x4;
typedef __attribute__((ext_vector_type(2))) u32 u32x2;
#define LAS __attribute__((address_space(3)))
#define GAS __attribute__((address_space(1)))
#define DEV __device__ __forceinline__
typedef __amdgpu_buffer_rsrc_t rsrc_t;

static constexpr int L = 2048, D = 512, NS = 128, F = 1792;

static constexpr size_t WS_H = 0;
static constexpr size_t WS_XT = 8u << 20;
static constexpr size_t WS_AO = 11u << 20;
static constexpr size_t WS_ST = 16u << 20;
static constexpr size_t WS_CS = WS_ST + (64u << 10);
static constexpr int FS = 128;
static constexpr size_t WS_FX = WS_ST + (128u << 10);
static constexpr size_t WS_FH = WS_FX + 32 * FS;
static constexpr size_t WS_EP = WS_FH + 4096 * FS;
static constexpr int H_BYTES = L * F * 2;
static constexpr int XT_BLK = 65536 + 256, XT_BYTES = 32 * XT_BLK;
static constexpr int AO_ROW = 16384 + 128, AO_BYTES = 256 * AO_ROW;
static constexpr u32 OOB = 0x7fffffffu;

static constexpr int SLOT = 49152;
static constexpr int LDS_TOTAL = 163840;
static constexpr int SCR = 2 * SLOT;
static constexpr int LDS_SC = LDS_TOTAL - 64;
static constexpr int KG_ROW = 112, KG_PLANE = 128 * KG_ROW;
static constexpr int LDS_KD = SCR + 4 * KG_PLANE;
static constexpr int U_COPY = 4160;
static constexpr int U_CH = 2 * U_COPY;
static constexpr int LDS_U = SCR;
static constexpr int KX_STRIDE = 4608;
static constexpr int LDS_KX = SCR + 2 * U_CH;

struct Params {
  const u16 *x, *lar, *aim, *cre, *cim, *ldt, *dsk, *w1, *w3, *w2;
  u16* out;
  char* ws;
};

DEV float asf(u32 v) { return __builtin_bit_cast(float, v); }
DEV u32 asu(float v) { return __builtin_bit_cast(u32, v); }
DEV float bf2f(u32 v) { return asf(v << 16); }
DEV float bfhi(u32 v) { return asf(v & 0xffff0000u); }
DEV u32 pk_bf16(float a, float b) { f32x2 p = {a, b}; return __builtin_bit_cast(u32, __builtin_convertvector(p, bf16x2)); }
DEV u32 pk_f16(float a, float b) { f32x2 p = {a, b}; return __builtin_bit_cast(u32, __builtin_convertvector(p, f16x2)); }
DEV float rbf(float a) { return bf2f(pk_bf16(a, a)); }
DEV float h2f(u32 v) { return (float)__builtin_bit_cast(_Float16, (u16)v); }
DEV bool all_of(bool p) { return __builtin_amdgcn_ballot_w64(!p) == 0; }
#define FMA __builtin_fmaf
#define AI __attribute__((always_inline))
template <int I> struct IC { constexpr operator int() const { return I; } };
template <int B, int E, typename FN> DEV void sfor(FN&& f) { if constexpr (B < E) { f(IC<B>{}); sfor<B + 1, E>(f); } }

template <typename T> DEV T lds_ld(u32 a) { return *(const LAS T*)a; }
template <typename T> DEV void lds_st(u32 a, T v) { *(LAS T*)a = v; }
DEV u32x2 lds_tr(u32 a) { return __builtin_bit_cast(u32x2, __builtin_amdgcn_ds_read_tr16_b64_v4i16((LAS s16x4*)a)); }
DEV u32x4 cat(u32x2 a, u32x2 b) { return __builtin_shufflevector(a, b, 0, 1, 2, 3); }
DEV f32x16 mma32(u32x4 a, u32x4 b, f32x16 c) { return __builtin_amdgcn_mfma_f32_32x32x16_bf16(__builtin_bit_cast(bf16x8, a), __builtin_bit_cast(bf16x8, b), c, 0, 0, 0); }
DEV f32x4 mma_bf(u32x4 a, u32x4 b, f32x4 c) { return __builtin_amdgcn_mfma_f32_16x16x32_bf16(__builtin_bit_cast(bf16x8, a), __builtin_bit_cast(bf16x8, b), c, 0, 0, 0); }
DEV f32x4 mma_h(u32x4 a, u32x4 b, f32x4 c) { return __builtin_amdgcn_mfma_f32_16x16x32_f16(__builtin_bit_cast(f16x8, a), __builtin_bit_cast(f16x8, b), c, 0, 0, 0); }
DEV rsrc_t mk_rsrc(const void* p, u32 bytes) { return __builtin_amdgcn_make_buffer_rsrc((void*)p, 0, bytes, 0x00020000); }
DEV void dma16(rsrc_t rs, u32 voff, u32 sbase, int simm, u32 lbase, int limm) {
  u32 t;
  asm volatile("s_add_u32 m0, %1, %2\n\ts_add_u32 %0, %3, %4\n\tbuffer_load_dwordx4 %5, %6, %0 offen lds"
               : "=&s"(t) : "s"(lbase), "n"(limm), "s"(sbase), "n"(simm), "v"(voff), "s"(rs) : "memory", "scc");
}
#define PINS(x) asm volatile("" : "+s"(x))
#define WAIT_V(n) asm volatile("s_waitcnt vmcnt(%0)" :: "n"(n) : "memory")
#define BAR() asm volatile("s_waitcnt lgkmcnt(0)\n\ts_barrier" ::: "memory")
#define SCHED() __builtin_amdgcn_sched_barrier(0)
#define PIN(x) asm volatile("" : "+v"(x))
#define PIN2(x, y) asm volatile("" : "+v"(x), "+v"(y))
#define RLX __ATOMIC_RELAXED, __HIP_MEMORY_SCOPE_AGENT
DEV u64 ld_agent(const char* p) { return __hip_atomic_load((GAS u64*)p, RLX); }
DEV void st_agent(char* p, u64 v) { __hip_atomic_store((GAS u64*)p, v, RLX); }
static constexpr u32 CODE_BYTES = 25 * 1024;

#define G2PRIO 0
static constexpr u32 SPIN_CAP = 1u << 22;
#define CHKC 64
#define CHK0 40

#define STAMP(i) do {} while (0)
#define STAMP_DECL
#define CSTAMP(i)
#define STAMP_OUT

DEV void wait_flags(const char* p, u64 tag) {
#pragma unroll 1
  for (u32 s = 0; s < SPIN_CAP; ++s) {
    u64 v = ld_agent(p);
    if (all_of(v == tag)) break;
    __builtin_amdgcn_s_sleep(1);
  }
  asm volatile("" ::: "memory");
}
DEV u64 ld8_async(const char* p) {
  u64 v;
  asm volatile("global_load_dwordx2 %0, %1, off sc1" : "=v"(v) : "v"(p) : "memory");
  return v;
}
DEV u32x4 ld16_async(rsrc_t rs, u32 voff, u32 soff) {
  u32x4 v;
  asm volatile("buffer_load_dwordx4 %0, %1, %2, %3 offen sc1" : "=v"(v) : "v"(voff), "s"(rs), "s"(soff) : "memory");
  return v;
}
DEV u32 ld4_async(const u16* base, u32 off) {
  u32 v;
  asm volatile("global_load_dword %0, %1, %2" : "=v"(v) : "v"(off), "s"(base) : "memory");
  return v;
}
template <int C> DEV float dppf(float v) { return asf((u32)__builtin_amdgcn_update_dpp(0, (int)asu(v), C, 0xf, 0xf, true)); }
DEV float row_sum(float v) { v += dppf<0x128>(v); v += dppf<0x124>(v); v += dppf<0x122>(v); v += dppf<0x121>(v); return v; }
DEV float row_max(float v) { v = __builtin_fmaxf(v, dppf<0x128>(v)); v = __builtin_fmaxf(v, dppf<0x124>(v)); v = __builtin_fmaxf(v, dppf<0x122>(v)); v = __builtin_fmaxf(v, dppf<0x121>(v)); return v; }
DEV float fold32(float a, float b) {
  auto r = __builtin_amdgcn_permlane32_swap(asu(a), asu(b), false, false);
  return asf(r[0]) + asf(r[1]);
}
DEV float fold16(float a, float b) {
  auto r = __builtin_amdgcn_permlane16_swap(asu(a), asu(b), false, false);
  return asf(r[0]) + asf(r[1]);
}
DEV float dot2bf(u32 a, u32 b, float c) { return __builtin_amdgcn_fdot2_f32_bf16(__builtin_bit_cast(bf16x2, a), __builtin_bit_cast(bf16x2, b), c, false); }
DEV u32 rdu(float v, int l) { return (u32)__builtin_amdgcn_readlane((int)asu(v), l); }
DEV float rdl(float v, int l) { return asf(rdu(v, l)); }
DEV u32 umax(u32 a, u32 b) { return a > b ? a : b; }
DEV u64 mk_tag(u32& misc) {
  PIN(misc);
  const u32 lo = (u32)__builtin_amdgcn_readlane((int)misc, 2), hi = (u32)__builtin_amdgcn_readlane((int)misc, 3);
  return (((u64)hi << 32) | lo) + 0x9E3779B97F4A7C15ull;
}

struct cpx { float r, i; };
DEV cpx cmul(cpx a, cpx b) { return cpx{FMA(a.r, b.r, -(a.i * b.i)), FMA(a.r, b.i, a.i * b.r)}; }
DEV cpx csqr(cpx a) { return cpx{FMA(a.r, a.r, -(a.i * a.i)), (a.r + a.r) * a.i}; }
DEV cpx cexp_at(float ar2, float airev, float p) {
  float e = __builtin_amdgcn_exp2f(ar2 * p);
  float fr = __builtin_amdgcn_fractf(airev * p);
  return cpx{e * __builtin_amdgcn_cosf(fr), e * __builtin_amdgcn_sinf(fr)};
}
struct KgIn { u32 lar, aim, cr, ci, misc; };
struct KgSeed { cpx v[32], m[3], qa[2], qb[2], C, s1, w512; float inv, dsk, dt, ar2, airev, mx, sm; };
struct KdTab { u16 v[2048]; };
constexpr KdTab make_kd() {
  KdTab t{};
  for (int i = 0; i < 2048; ++i) {
    int l = (i & ~7) + 7 - (i & 7);
    if (l == 0) l = 1;
    int sh = l < 256 ? 0 : l < 512 ? 1 : l < 1024 ? 2 : 3;
    int q = l >> sh, rem = l - (q << sh), half = (1 << sh) >> 1;
    if (sh > 0 && (rem > half || (rem == half && (q & 1)))) ++q;
    int p = (q << sh) - 1;
    int I = p >> 7, sg = I < 2 ? 0 : I < 4 ? 1 : I < 8 ? 2 : 3;
    int r = 8 * sg + (I << (3 - sg)) + ((p & 7) >> sg);
    t.v[i] = (u16)((r * 16 + ((p >> 3) & 15)) * 2);
  }
  return t;
}
__constant__ __attribute__((aligned(16))) KdTab kd_tab = make_kd();

#define HW 12
static constexpr int PD = 4;
constexpr int g2_k(int s) { return s < 4 ? 24 + s : s - 4; }
constexpr int g2_extra(int s) { return (s < HW ? 2 : 0) + (s > 20 ? s - 20 : 0); }
#define RATE 1
constexpr int g2_wait_hf(int s) { return (HW + RATE * s + RATE < 28 ? HW + RATE * s + RATE : 28) - (s + 1) + g2_extra(s); }

__global__ __launch_bounds__(512, 2) void fused(Params P) {
  int b = __builtin_amdgcn_workgroup_id_x(), tid = __builtin_amdgcn_workitem_id_x();
  int w = __builtin_amdgcn_readfirstlane(tid >> 6);
  STAMP_DECL;
  STAMP(0); CSTAMP(58);
  u32 vx0 = ((tid & 63) >> 3) * 1024 + (((tid & 7) ^ ((tid >> 4) & 3)) * 16);
  u32 vx1 = vx0 ^ 64;
  u32 vw = ((tid >> 4) & 3) * (F * 2) + (((tid & 15) ^ (((tid >> 4) & 3) << 2)) * 16);
  u32 sx0 = (u32)((((b & 3) * 4 + ((b >> 3) & 3)) * 128 + 16 * w) * 1024), sw0 = (u32)(16 * (w & 3) * (F * 2) + (((b >> 2) & 1) * 7 + (b >> 5)) * 256);
  u32 lx0 = w * 2048, lw0 = w * 4096;
  PIN2(vx0, vx1); PIN(vw); PINS(sx0); PINS(sw0); PINS(lx0); PINS(lw0);
  SCHED();
  rsrc_t rx = mk_rsrc(P.x, L * D * 2);
  rsrc_t rw = mk_rsrc(w >= 4 ? P.w3 : P.w1, D * F * 2);
  auto g1_piece = [&](int s, int i) AI {
    const int slot = ((s + 1) % 3) * SLOT;
    if (i == 0) dma16(rx, vx0, sx0, s * 128, lx0, slot);
    else if (i == 1) dma16(rx, vx1, sx0, s * 128 + 8192, lx0, slot + 1024);
    else dma16(rw, vw, sw0, (s * 64 + (i - 2) * 4) * F * 2, lw0, slot + 16384 + (i - 2) * 1024);
  };
  if (__builtin_expect(b < 224, 1)) {
    STAMP(12);
#pragma unroll
    for (int i = 0; i < 6; ++i) g1_piece(0, i);
  } else {
#pragma unroll
    for (int i = 0; i < 8; ++i) dma16(rx, (tid & 63) * 16, (u32)(((b & 3) * 8 + ((b >> 2) & 1) * 4 + ((b >> 3) & 3)) * 64 + 8 * w) * 1024, i * 1024, w * 8192, 1024 + i * 1024);
  }
  {
    u64 pc;
    asm volatile("s_getpc_b64 %0" : "=s"(pc));
    dma16(mk_rsrc((const void*)(pc & ~(u64)255), CODE_BYTES), w == 0 ? (b >> 3) * 1024 + tid * 16 : OOB, 0, 0, 0, 0);
  }
  PIN(tid); PINS(b); PINS(w);
  const int xcd = b & 7, idx = b >> 3, lane = tid & 63;
  KgIn kin;
  const bool is_aux = idx >= 28;
  const int mb = (xcd & 3) * 4 + (idx & 3), nb = (xcd >> 2) * 7 + (idx >> 2);
  const int tt = xcd * 4 + (idx & 3), nb2 = ((idx >> 2) + 4 - (int)((size_t)P.w2 >> 7)) & 7;
  const int th = w & 1, fq = w >> 1;
  const int ch = w >> 2, v4 = w & 3;
  const int lc = lane & 15, lg = lane >> 4, lq = (lane & 15) >> 2, lp = lane & 3, lh = lane >> 5;
  u32 a_u = LDS_U + ch * U_CH + 32 + (tid & 255) * 16, a_kx = LDS_KX + ch * KX_STRIDE + 512 + (tid & 255) * 16;
  u32 a_z = (tid & 255) < 4 ? LDS_U + ch * U_CH + ((tid + 3) & 1) * 16 + (((tid + 3) >> 1) & 1) * U_COPY : LDS_KX + ch * KX_STRIDE + (tid & 31) * 16;
  const int s00 = 16 * (lg >> 1) + lc - 8 * (lg & 1) - 7;
  u32 abase = LDS_U + ch * U_CH + (s00 & 1) * U_COPY + 32 + (s00 & ~1) * 2 + v4 * 128;
  u32 bbase = LDS_KX + ch * KX_STRIDE + 512 + (16 * (lc - (lg >> 1)) + 8 * (lg & 1)) * 2 - v4 * 128 - 64;
  u32 o_fh = ((tt * 2 + ch) * 64 + 14 * v4 + (lane < 14 ? lane : 0)) * FS;
  u32 o_pub = lane == 0 ? (u32)(((mb * 4 + th * 2) * 64 + nb * 4 + fq) * FS) : OOB - 64 * FS;
  PIN2(a_u, a_kx); PIN2(a_z, abase); PIN2(bbase, o_fh); PIN(o_pub);
  auto small_loads = [&](int k) AI {
    const u32 o = (2 * b + ch) * (NS * 2) + lane * 4;
    if (k == 0) {
      const u64 p0 = (u64)(P.ldt + 2 * b), p1 = (u64)(P.dsk + 2 * b), p2 = (u64)(P.ws + WS_EP);
      u32x2 p = {(u32)p2 + (lane & 1) * 4, (u32)(p2 >> 32)};
      asm volatile("v_writelane_b32 %0, %2, 0\n\tv_writelane_b32 %1, %3, 0\n\tv_writelane_b32 %0, %4, 1\n\tv_writelane_b32 %1, %5, 1"
                   : "+v"(p[0]), "+v"(p[1]) : "s"((u32)p0), "s"((u32)(p0 >> 32)), "s"((u32)p1), "s"((u32)(p1 >> 32)));
      asm volatile("global_load_dword %0, %1, off sc1" : "=v"(kin.misc) : "v"(p) : "memory");
    }
    if (k == 1) { kin.lar = ld4_async(P.lar, o); kin.aim = ld4_async(P.aim, o); }
    if (k == 2) { kin.cr = ld4_async(P.cre, o); kin.ci = ld4_async(P.cim, o); }
  };
  u32 pulled;
  auto pull = [&]() AI {
    asm volatile("buffer_load_dword %0, %1, %2, 0 offen" : "=v"(pulled) : "v"(lane < 7 ? (u32)((b * 8 + w) * 7 + lane) * 128 : OOB), "s"(mk_rsrc(P.w2, F * D * 2)) : "memory");
  };
  u64 tag; u32 t32;
  KgSeed kg;
  rsrc_t rw2 = mk_rsrc(P.w2, F * D * 2);
  const u32 vw2 = (lane >> 3) * 1024 + (((lane & 7) ^ ((lg & 1) << 2)) * 16);
  u32 sw2 = (u32)((v4 * 448 + ch * 8) * 1024 + nb2 * 128), lw2 = v4 * 2048 + ch * 1024;
  PINS(sw2); PINS(lw2);
  auto w2_piece = [&](int k, int slot) AI { dma16(rw2, vw2, sw2, k * 16384, lw2, slot + (k < 24 ? k % 6 : k & 1) * 8192); };
  rsrc_t rxt = mk_rsrc(P.ws + WS_XT, XT_BYTES);

  auto pinc = [&](cpx& c) AI { PIN2(c.r, c.i); };
  auto sel = [&](cpx a, cpx b2) AI { return cpx{th ? a.r : b2.r, th ? a.i : b2.i}; };
  auto seed_piece = [&](int k) AI {
    const u32 msh = ((w >> 1) & 1) ? 0 : 16, csh = ch ? 0 : 16;
    if (k == 0) {
      asm volatile("" : "+v"(kin.lar), "+v"(kin.aim), "+v"(kin.cr), "+v"(kin.ci));
      kg.dt = rbf(__builtin_amdgcn_exp2f(bfhi((u32)__builtin_amdgcn_readlane((int)kin.misc, 0) << csh) * 1.44269504088896f)); PIN(kg.dt);
    }
    if (k == 1) { kg.ar2 = (-rbf(__builtin_amdgcn_exp2f(bfhi(kin.lar << msh) * 1.44269504088896f)) * kg.dt) * 1.44269504088896f; PIN(kg.ar2); }
    if (k == 2) {
      kg.airev = (bfhi(kin.aim << msh) * kg.dt) * 0.159154943091895f;
      kg.C = cpx{bfhi(kin.cr << msh), bfhi(kin.ci << msh)}; PIN(kg.airev); pinc(kg.C);
    }
    if (k == 3) { kg.mx = dot2bf(kin.ci & 0x7fff7fffu, 0x3f803f80u, dot2bf(kin.cr & 0x7fff7fffu, 0x3f803f80u, 0.f)); kg.sm = dot2bf(kin.cr, 0x3f803f80u, 0.f); PIN2(kg.mx, kg.sm); }
    if (k == 4) { kg.sm = fold32(kg.sm, kg.mx); kg.sm = fold16(kg.sm, kg.sm); }
    if (k == 5) { kg.sm += dppf<0x128>(kg.sm); kg.sm += dppf<0x124>(kg.sm); }
    if (k == 6) { kg.sm += dppf<0x122>(kg.sm); kg.sm += dppf<0x121>(kg.sm); kg.sm *= 2.f * kg.dt; }
    if (k >= 4 && k <= 6) PIN(kg.sm);
    if (k == 7) {
      kg.dsk = bfhi((u32)__builtin_amdgcn_readlane((int)kin.misc, 1) << csh) + rdl(kg.sm, 0);
      const u32 se = 268u - ((rdu(kg.sm, 32) >> 23) & 0xff);
      kg.mx = asf((se > 254u ? 254u : se) << 23); PIN(kg.dsk);
    }
    if (k == 8) {
      kg.inv = asf((254u << 23) - asu(kg.mx));
      const float cs = 2.f * kg.dt;
      kg.C.r = kg.C.r * cs * kg.mx; kg.C.i = kg.C.i * cs * kg.mx; PIN(kg.inv); pinc(kg.C);
    }
    if (k == 9) { kg.qa[1] = cexp_at(kg.ar2, kg.airev, 128.f); pinc(kg.qa[1]); }
    if (k == 10) { kg.qb[1] = csqr(kg.qa[1]); pinc(kg.qb[1]); }
    if (k == 11) { kg.w512 = csqr(kg.qb[1]); kg.qb[0] = sel(kg.qa[1], kg.w512); pinc(kg.qb[0]); }
    if (k == 12) { kg.s1 = cexp_at(kg.ar2, kg.airev, th ? 1.f : 8.f); kg.s1.i = th ? kg.s1.i : -kg.s1.i; pinc(kg.s1); }
    if (k == 13) { kg.m[0] = csqr(kg.s1); pinc(kg.m[0]); }
    if (k == 14) { kg.m[1] = csqr(kg.m[0]); kg.qa[0] = sel(kg.m[1], kg.qb[1]); pinc(kg.m[1]); pinc(kg.qa[0]); }
    if (k == 15) { kg.m[2] = sel(kg.qa[1], csqr(kg.m[1])); pinc(kg.m[2]); }
    if (k == 16) { kg.v[0] = sel(cmul(kg.C, kg.s1), cpx{1.f, 0.f}); pinc(kg.v[0]); }
    if (k == 17) { kg.v[1] = cmul(kg.v[0], kg.s1); pinc(kg.v[1]); }
    if (k == 18) { kg.v[16] = cexp_at(kg.ar2, kg.airev, th ? 258.f : 1032.f); pinc(kg.v[16]); }
    if (k == 19) { kg.v[16] = cmul(kg.v[16], kg.C); pinc(kg.v[16]); }
    if (k == 20) { kg.v[17] = cmul(kg.v[16], sel(kg.m[0], kg.qa[1])); pinc(kg.v[17]); }
    if (k == 21) { kg.v[24] = cmul(kg.v[17], kg.qb[1]); kg.v[25] = cmul(kg.v[24], kg.m[1]); pinc(kg.v[24]); pinc(kg.v[25]); }
  };
  u32 kre[16], kim[16];
  auto kx_mul = [&](int d, int s, cpx m) AI { kg.v[d] = cmul(kg.v[s], m); PIN2(kg.v[d].r, kg.v[d].i); };
  auto kx_pack = [&](int p) AI { kre[p] = pk_f16(kg.v[2 * p].r, kg.v[2 * p + 1].r); kim[p] = pk_f16(kg.v[2 * p].i, kg.v[2 * p + 1].i); PIN2(kre[p], kim[p]); };
  auto kx_piece = [&](int k) AI {
    if (k < 20) {
      const int q = k / 10, j = k % 10, o = 16 + 8 * q;
      if (j < 2) kx_mul(o + 2 + j, o + j, kg.qa[q]);
      else if (j < 6) kx_mul(o + 2 + j, o + j - 2, kg.qb[q]);
      else kx_pack(o / 2 + j - 6);
    } else {
      const int j = k - 20;
      if (j < 2) kx_mul(2 + j, j, kg.m[0]);
      else if (j < 6) kx_mul(2 + j, j - 2, kg.m[1]);
      else {
        const int p = (j - 6) >> 2, r = (j - 6) & 3;
        if (r < 2) kx_mul(8 + 2 * p + r, 2 * p + r, kg.m[2]);
        else kx_pack(p + 4 * (r - 2));
      }
    }
  };
  auto kt_store = [&](int i) AI {
    const int c = i >> 1;
    u32 a = SCR + ch * 2 * KG_PLANE + (64 * ((w >> 1) & 1) + lane) * KG_ROW + (th ? c * 16 : (c < 2 ? 80 + c * 16 : 64)) + (i & 1) * KG_PLANE;
    if (c == 3) a = th ? a : LDS_KD + 4096 + lane * 16;
    const u32* p = (i & 1) ? kim : kre;
    lds_st<u32x4>(a, u32x4{p[4 * c], p[4 * c + 1], p[4 * c + 2], p[4 * c + 3]});
  };
  const int kt = v4 < 3 ? v4 : 0;
  const u32 krb = SCR + ch * 2 * KG_PLANE + (8 * lg + lq) * KG_ROW + lp * 8, kra = krb + kt * 32;
  u32x4 kA[8], kB[8];
  f32x4 ka = {0.f, 0.f, 0.f, 0.f}, kb = ka;
  auto kt_rd = [&](int k) AI {
    const u32 o = k * 32 * KG_ROW;
    kA[k] = cat(lds_tr(kra + o), lds_tr(kra + o + 4 * KG_ROW)); kB[k] = cat(lds_tr(krb + 80 + o), lds_tr(krb + 80 + o + 4 * KG_ROW));
  };
  auto kt_mma = [&](int k) AI { if (k & 1) kb = mma_h(kB[k], kA[k], kb); else ka = mma_h(kB[k], kA[k], ka); };
  auto kt_put = [&]() AI {
    ka += kb;
    lds_st<f32x2>(LDS_SC + ch * 8, f32x2{kg.inv, kg.dsk});
    lds_st<u32x2>(LDS_KD + ch * 1536 + ((16 * kt + lc) * 16 + 4 * lg) * 2, u32x2{pk_f16(ka[0], ka[1]), pk_f16(ka[2], ka[3])});
  };
  u32x4 uv, kdv; u32 unx, kgv[8];
  const int g8 = tid & 255;
  auto load_u = [&]() AI {
    const u32 cb = (2 * b + ch) * 128, gn = g8 + 1;
    uv = ld16_async(rxt, (g8 >> 3) * XT_BLK + (g8 & 7) * 16 + cb, 0);
    asm volatile("buffer_load_dword %0, %1, %2, 0 offen sc1" : "=v"(unx) : "v"((gn >> 3) * XT_BLK + (gn & 7) * 16 + cb), "s"(rxt) : "memory");
    asm volatile("global_load_dwordx4 %0, %1, %2" : "=v"(kdv) : "v"(g8 * 16), "s"(&kd_tab) : "memory");
  };
  auto kt_fin = [&](int k) AI {
    if (k == 0) { PIN(uv); PIN(unx); PIN(kdv); }
    if (k < 4) { kgv[2 * k] = lds_ld<u16>(LDS_KD + ch * 1536 + (kdv[k] & 0xffffu)); kgv[2 * k + 1] = lds_ld<u16>(LDS_KD + ch * 1536 + (kdv[k] >> 16)); }
    if (k == 4) lds_st<u32x4>(a_u, uv);
    if (k == 5) lds_st<u32x4>(a_u + U_COPY,
                              u32x4{__builtin_amdgcn_alignbit(uv[1], uv[0], 16), __builtin_amdgcn_alignbit(uv[2], uv[1], 16),
                                    __builtin_amdgcn_alignbit(uv[3], uv[2], 16), __builtin_amdgcn_alignbit(unx, uv[3], 16)});
    if (k == 6) {
      lds_st<u32x4>(a_z, u32x4{0, 0, 0, g8 == 0 ? uv[0] << 16 : 0u});
    }
    if (k == 7) {
      asm volatile("" : "+v"(kgv[0]), "+v"(kgv[1]), "+v"(kgv[2]), "+v"(kgv[3]), "+v"(kgv[4]), "+v"(kgv[5]), "+v"(kgv[6]), "+v"(kgv[7]));
      lds_st<u32x4>(a_kx, u32x4{kgv[0] | (kgv[1] << 16), kgv[2] | (kgv[3] << 16), kgv[4] | (kgv[5] << 16), kgv[6] | (g8 == 0 ? 0u : kgv[7] << 16)});
    }
  };
  const char* pfx = P.ws + WS_FX + (4 * w + lp) * FS;

  f32x16 ag[2], au[2];
#pragma unroll
  for (int i = 0; i < 16; ++i) { ag[0][i] = 0.f; ag[1][i] = 0.f; au[0][i] = 0.f; au[1][i] = 0.f; }
  float eke[2] = {1.f, 1.f}, es2[2] = {1.f, 1.f};
  rsrc_t rhs = mk_rsrc(P.ws + WS_H, H_BYTES);
  const u32 ho = (u32)(((mb * 4 + th * 2) * 112 + nb * 8 + fq * 2) * 1024 + lane * 16);
  float ee[4], eg[4]; u32x4 eo;
  auto epi_piece = [&](int t2, int k) AI {
    const int q = k / 5, j = k % 5;
#pragma unroll
    for (int i = 0; i < 4; ++i) {
      if (j == 0) { ee[i] = ag[t2][4 * q + i] * eke[t2]; eg[i] = ag[t2][4 * q + i] * au[t2][4 * q + i]; }
      if (j == 1) ee[i] = __builtin_amdgcn_exp2f(ee[i]);
      if (j == 2) ee[i] = FMA(ee[i], es2[t2], es2[t2]);
      if (j == 3) ee[i] = __builtin_amdgcn_rcpf(ee[i]);
      if (j == 4) eg[i] *= ee[i];
    }
    if (j < 4) asm volatile("" : "+v"(ee[0]), "+v"(ee[1]), "+v"(ee[2]), "+v"(ee[3]), "+v"(eg[0]), "+v"(eg[1]), "+v"(eg[2]), "+v"(eg[3]));
    if (j == 4) {
      eo[2 * (q & 1)] = pk_bf16(eg[0], eg[1]); eo[2 * (q & 1) + 1] = pk_bf16(eg[2], eg[3]);
      if (q & 1) __builtin_amdgcn_raw_buffer_store_b128(eo, rhs, ho + (t2 * 112 + (q >> 1)) * 1024, 0, 16);
      else PIN2(eo[0], eo[1]);
    }
  };

  u32 w2a[2];
  w2a[0] = 3 * SLOT + v4 * 2048 + (8 * lh + lq) * 128 + (4 * ((lg ^ v4) & 1) + ((lp & 1) << 1) + (lp >> 1)) * 8 + (((lq >> 1) ^ (v4 >> 1)) & 1) * 64;
  w2a[1] = w2a[0] ^ 64;
  u32x4 wr[28][2];
  auto w2_rd = [&](int i) AI { const int k = i >> 1; const u32 a = w2a[i & 1] + (k < 24 ? k % 6 : k & 1) * 8192; wr[k][i & 1] = cat(lds_tr(a), lds_tr(a + 512)); };
  auto w2_base = [&](int dl) AI { w2a[0] += dl; w2a[1] += dl; PIN2(w2a[0], w2a[1]); };
  rsrc_t rfh = mk_rsrc(P.ws + WS_FH, is_aux ? 0 : 4096 * FS);
  auto publish = [&](int t2) AI { __builtin_amdgcn_raw_buffer_store_b64(u32x2{t32, (u32)(tag >> 32)}, rfh, o_pub, t2 * 64 * FS, 16); };

  auto window = [&](auto EPI) AI {
    STAMP(5); CSTAMP(60);
    BAR();
    STAMP(32);
    sfor<0, 20>([&](auto I) AI {
      constexpr int k = I;
      SCHED();
      if (k < 8) kt_rd(k); else w2_rd(4 + k);
      if (k >= 3 && k < 11) kt_mma(k - 3);
      if (k == 14) kt_put();
      if (k == 7) { WAIT_V(2); if (EPI) publish(0); }
      if (k % 3 == 1 && k < 18) w2_piece(12 + k / 3, 0);
      SCHED();
      if (EPI) epi_piece(1, k);
    });
  };

  if (__builtin_expect(!is_aux, 1)) {
    STAMP(13);
    BAR();
#pragma unroll
    for (int i = 0; i < 6; ++i) g1_piece(1, i);
    STAMP(2); CSTAMP(59);
    u32 xa[4], wa;
    {
      const int row = 64 * th + (lane & 31);
#pragma unroll
      for (int ks = 0; ks < 4; ++ks) xa[ks] = SLOT + row * 128 + (((2 * ks + lh) ^ ((row >> 1) & 7)) * 16);
      wa = SLOT + 16384 + (8 * lh + lq) * 256 + ((fq ^ lq) * 64) + (4 * (lg & 1) + ((lp & 1) << 1) + (lp >> 1)) * 8;
    }
    const int G = (lane & 31) >> 2;
    const char* pcs = P.ws + WS_CS + (size_t)(lh * F + nb * 128 + fq * 32 + (4 * (G >> 2) + 2 * (G & 1) + ((G >> 1) & 1)) * 4 + lp) * 8;
    const char* pst = P.ws + WS_ST + (size_t)(mb * 128 + 64 * th + lane) * 8;
    u64 gc, gs, fx; u32 gst[2];
    u32x4 xg, xu, xm[2];
    WAIT_V(6);
    STAMP(15);
    BAR();
    STAMP(3);
    u32x4 fx0[4], fx1[4], fg[4], fu[4];
    auto rd0 = [&](int d, int ks) AI { fx0[d] = lds_ld<u32x4>(xa[ks]); fg[d] = cat(lds_tr(wa + ks * 4096), lds_tr(wa + ks * 4096 + 1024)); };
    auto rd1 = [&](int d, int ks) AI { fu[d] = cat(lds_tr(wa + 16384 + ks * 4096), lds_tr(wa + 16384 + ks * 4096 + 1024)); fx1[d] = lds_ld<u32x4>(xa[ks] + 4096); };
    rd0(0, 0); rd1(0, 0);
    g1_piece(2, 0); g1_piece(2, 1);
    auto filler = [&](int n) AI {
      if (n == 8) pull();
      if (n >= 9 && n < 12) small_loads(n - 9);
      if (n == 28) { PIN(pulled); tag = mk_tag(kin.misc); t32 = (u32)tag; }
      if (n >= 29 && n < 58 && n % 4 != 0) seed_piece(n - 29 - (n - 29) / 4);
      if (n >= 58 && n < 110 && n % 5 != 2) kx_piece(n - 58 - (n - 58) / 5);
      if (n == 26 || n == 27) w2_piece(24 + n - 26, 3 * SLOT);
      if (n >= 48 && n < 60 && n % 4 == 0) { w2_rd(48 + (n - 48) / 4); if (n == 56) w2_rd(51); }
      if (n == 74 || n == 75) w2_piece(26 + n - 74, 3 * SLOT);
      if (n >= 97 && n < 113 && n % 5 == 2) w2_rd(52 + (n - 97) / 5);
      if (n == 111) { w2_rd(55); w2_base(-3 * SLOT); }
      if (n == 92) fx = ld8_async(pfx);
      if (n == 93) { gc = ld8_async(pcs); gs = ld8_async(pst); }
      if (n == 120) {
        WAIT_V(10);
        PIN(gc); PIN(gs); PIN(fx);
        auto ok = [&]() AI { return all_of((((u32)(gc >> 32) ^ t32) | ((u32)(gs >> 32) ^ t32) | ((u32)fx ^ t32) | ((u32)(fx >> 32) ^ (u32)(tag >> 32))) == 0); };
        if (__builtin_expect(!ok(), 0)) {
#pragma unroll 1
          for (u32 sp = 0; sp < SPIN_CAP; ++sp) {
            gc = ld_agent(pcs); gs = ld_agent(pst); fx = ld_agent(pfx);
            if (ok()) break;
            __builtin_amdgcn_s_sleep(1);
          }
        }
      }
      if (n == 121) {
        const float cv = asf((u32)gc);
        const float c0 = rbf(cv), r1 = cv - c0, c1 = rbf(r1);
        const u32 e0 = pk_bf16(c0, c1), e1 = pk_bf16(r1 - c1, 0.f);
        xg = u32x4{lh ? 0u : e0, lh ? 0u : e1, 0, 0}; xu = u32x4{lh ? e0 : 0u, lh ? e1 : 0u, 0, 0};
        PIN2(xg, xu);
        auto r = __builtin_amdgcn_permlane32_swap((u32)gs, (u32)gs, false, false);
        gst[0] = r[0]; gst[1] = r[1];
      }
      if (n == 122 || n == 123) {
        const int t = n - 122;
        const u32 m = (gst[t] & 0xffffu) ^ 0x8000u;
        xm[t] = u32x4{m | (m << 16), m, 0, 0};
        const float sd = bfhi(gst[t]);
        eke[t] = -1.44269504088896f * __builtin_amdgcn_rcpf(sd); es2[t] = sd * sd;
        PIN(xm[t]); PIN2(eke[t], es2[t]);
      }
    };
    sfor<0, 28>([&](auto I) AI {
      constexpr int s = I / 4, ks = I % 4;
      if (ks == 3) {
        STAMP(16 + 2 * s);
        if (s == 0) WAIT_V(12); else if (s == 1 || s == 4) WAIT_V(8); else if (s == 6) WAIT_V(9); else if (s < 7) WAIT_V(6);
        BAR();
        if (s < 7) {
          const int dl = (s + 2) % 3 == 0 ? -2 * SLOT : SLOT;
#pragma unroll
          for (int i = 0; i < 4; ++i) { xa[i] += dl; PIN(xa[i]); }
          wa += dl; PIN(wa);
        }
        STAMP(17 + 2 * s);
      }
      const int n = s * 16 + ks * 4, kn = (ks + 1) & 3;
      const int rs = ks == 3 ? s : s - 1, pi = ks == 3 ? 0 : 2 * ks + 2;
      auto piece = [&](int i) AI {
        if (i > 5) return;
        if (rs + 3 < 8) g1_piece(rs + 3, i);
        else if (rs == 5) w2_piece(i, 0);
        else if (rs == 6) w2_piece(6 + i, SLOT);
      };
      SCHED(); ag[0] = mma32(fg[0], fx0[0], ag[0]); SCHED();
      rd0(1, kn);
      filler(n);
      SCHED(); au[0] = mma32(fu[0], fx0[0], au[0]); SCHED();
      rd1(1, kn);
      filler(n + 1);
      SCHED(); ag[1] = mma32(fg[0], fx1[0], ag[1]); SCHED();
      filler(n + 2); piece(pi);
      SCHED(); au[1] = mma32(fu[0], fx1[0], au[1]); SCHED();
      filler(n + 3); piece(pi + 1);
      SCHED();
      fx0[0] = fx0[1]; fx1[0] = fx1[1]; fg[0] = fg[1]; fu[0] = fu[1];
    });
    sfor<0, 4>([&](auto I) AI {
      constexpr int ks = I;
      SCHED(); ag[0] = mma32(fg[ks], fx0[ks], ag[0]); SCHED();
      if (ks < 3) rd0(ks + 1, ks + 1);
      if (ks == 0) { w2_piece(8, SLOT); w2_piece(9, SLOT); }
      if (ks == 1) filler(120);
      if (ks == 2) filler(122);
      if (ks == 3) { STAMP(30); WAIT_V(6); BAR(); STAMP(31); load_u(); }
      SCHED(); au[0] = mma32(fu[ks], fx0[ks], au[0]); SCHED();
      if (ks < 3) rd1(ks + 1, ks + 1);
      if (ks == 1) { filler(121); w2_piece(10, SLOT); w2_piece(11, SLOT); }
      if (ks == 2) filler(123);
    });
    SCHED(); ag[0] = mma32(xg, xm[0], ag[0]); SCHED(); kt_store(0);
    SCHED(); au[0] = mma32(xu, xm[0], au[0]); SCHED(); kt_store(1);
    sfor<0, 10>([&](auto I) AI {
      constexpr int i = I;
      SCHED();
      if (i < 8) { if (i & 1) au[1] = mma32(fu[i >> 1], fx1[i >> 1], au[1]); else ag[1] = mma32(fg[i >> 1], fx1[i >> 1], ag[1]); }
      else if (i == 8) ag[1] = mma32(xg, xm[1], ag[1]); else au[1] = mma32(xu, xm[1], au[1]);
      SCHED();
      if (i < 6) kt_store(2 + i);
      w2_rd(i);
      if (i > 0) { epi_piece(0, 2 * i - 2); epi_piece(0, 2 * i - 1); }
    });
    w2_rd(10); w2_rd(11); w2_base(SLOT);
    epi_piece(0, 18); epi_piece(0, 19);
    STAMP(4);
    WAIT_V(5);
    window(IC<1>{});
  } else {
    const int a = (xcd & 3) * 8 + (xcd >> 2) * 4 + (idx & 3), j16 = (xcd & 3) * 4 + (idx & 3), ca = j16 < 14 ? j16 : 13;
    const int cmat = ca >= 7, cnb = (xcd >> 2) * 7 + (cmat ? ca - 7 : ca);
    rsrc_t rc = mk_rsrc(cmat == 1 ? P.w3 : P.w1, D * F * 2);
    const u32 vo = (tid >> 4) * (F * 2) + cnb * 256 + lc * 16;
    u32x4 wv[16], xv[8];
    float s1[8], s2[8];
    small_loads(0); small_loads(1); small_loads(2);
    STAMP(18);
    WAIT_V(4);
#pragma unroll
    for (int i = 0; i < 8; ++i) xv[i] = lds_ld<u32x4>(1024 + w * 8192 + i * 1024 + lane * 16);
    pull();
    STAMP(19);
    tag = mk_tag(kin.misc); t32 = (u32)tag;
#pragma unroll
    for (int i = 0; i < 8; ++i) {
      s1[i] = 0.f; s2[i] = 0.f;
#pragma unroll
      for (int j = 0; j < 4; ++j) { s1[i] = dot2bf(xv[i][j], 0x3f803f80u, s1[i]); s2[i] = dot2bf(xv[i][j], xv[i][j], s2[i]); }
      wv[i] = __builtin_amdgcn_raw_buffer_load_b128(rc, vo, i * 32 * F * 2, 0);
      SCHED();
    }
    STAMP(20);
    float rs[2], mr[2];
#pragma unroll
    for (int g = 0; g < 2; ++g) {
      const float S = row_sum(fold16(fold32(s1[4 * g], s1[4 * g + 2]), fold32(s1[4 * g + 1], s1[4 * g + 3])));
      const float Q = row_sum(fold16(fold32(s2[4 * g], s2[4 * g + 2]), fold32(s2[4 * g + 1], s2[4 * g + 3])));
      const float mf = S * (1.f / D);
      const float var = rbf(__builtin_fmaxf(Q * (1.f / D) - mf * mf, 0.f));
      const float m = rbf(mf);
      const float sd = rbf(__builtin_amdgcn_sqrtf(rbf(var + 1e-5f)));
      rs[g] = __builtin_amdgcn_rcpf(sd);
      mr[g] = -m * rs[g];
      __builtin_amdgcn_raw_buffer_store_b64(u32x2{asu(sd) | (asu(m) >> 16), t32}, mk_rsrc(P.ws + WS_ST, L * 8), lc == 0 ? (u32)(64 * a + 8 * w + 4 * g + lg) * 8 : OOB, 0, 16);
    }
    STAMP(21);
    float rr[8], cc[8];
#pragma unroll
    for (int i = 0; i < 8; ++i) { rr[i] = rdl(rs[i >> 2], (i & 3) * 16); cc[i] = rdl(mr[i >> 2], (i & 3) * 16); }
    asm volatile("s_nop 1" : "+s"(rr[0]), "+s"(rr[1]), "+s"(rr[2]), "+s"(rr[3]), "+s"(rr[4]), "+s"(rr[5]), "+s"(rr[6]), "+s"(rr[7]));
#pragma unroll
    for (int e = 0; e < 8; ++e) {
      u32x4 o;
#pragma unroll
      for (int j = 0; j < 4; ++j) {
        const u32 v0 = xv[2 * j][e >> 1], v1 = xv[2 * j + 1][e >> 1];
        const float f0 = (e & 1) ? bfhi(v0) : bf2f(v0), f1 = (e & 1) ? bfhi(v1) : bf2f(v1);
        u32 d;
        asm("v_fma_mixlo_f16 %0, %1, %2, %3\n\tv_fma_mixhi_f16 %0, %4, %5, %6" : "=&v"(d) : "v"(f0), "s"(rr[2 * j]), "v"(cc[2 * j]), "v"(f1), "s"(rr[2 * j + 1]), "v"(cc[2 * j + 1]));
        o[j] = d;
      }
      lds_st<u32x4>((u32)(SCR + (8 * lane + e) * 128 + ((w ^ (lane & 7)) * 16)), o);
      wv[8 + e] = __builtin_amdgcn_raw_buffer_load_b128(rc, vo, (8 + e) * 32 * F * 2, 0);
      SCHED();
    }
    STAMP(23);
    BAR();
    STAMP(24);
#pragma unroll
    for (int j = 0; j < 8; ++j) {
      const int d = (tid >> 3) + 64 * j, sl = tid & 7;
      __builtin_amdgcn_raw_buffer_store_b128(lds_ld<u32x4>((u32)(SCR + d * 128 + ((sl ^ ((d >> 3) & 7)) * 16))), rxt, a * XT_BLK + d * 128 + sl * 16, 0, 16);
    }
    STAMP(25);
    u32x4 sel;
    const u32 one = (lc & 1) ? 0x3f800000u : 0x3f80u;
    sel[0] = (lc >> 1) == 0 ? one : 0; sel[1] = (lc >> 1) == 1 ? one : 0;
    sel[2] = (lc >> 1) == 2 ? one : 0; sel[3] = (lc >> 1) == 3 ? one : 0;
    f32x4 c0 = {0.f, 0.f, 0.f, 0.f}, c1 = c0;
#pragma unroll
    for (int k = 0; k < 16; k += 2) { c0 = mma_bf(sel, wv[k], c0); c1 = mma_bf(sel, wv[k + 1], c1); }
    c0 += c1; PIN(c0);
    STAMP(26);
    if (lane < 32) lds_st<f32x4>(SLOT + 32768 + w * 512 + lc * 32 + lg * 16, c0);
    STAMP(27);
    WAIT_V(0);
    STAMP(28);
    PIN(pulled);
    BAR();
    if (tid == 0) st_agent(P.ws + WS_FX + a * FS, tag);
    if (tid < 128) {
      float sum = 0.f;
#pragma unroll
      for (int j = 0; j < 8; ++j) sum += lds_ld<float>(SLOT + 32768 + j * 512 + tid * 4);
      st_agent(P.ws + WS_CS + (size_t)(cmat * F + cnb * 128 + tid) * 8, ((u64)t32 << 32) | asu(sum));
    }
    STAMP(2); CSTAMP(59);
    sfor<0, 64>([&](auto I) AI {
      if (I < 4) w2_piece(24 + I, I < 2 ? 3 * SLOT : 2 * SLOT); else if (I < 10) w2_piece(I - 4, 0);
      if (I == 12) BAR();
      if (I >= 12 && I < 18) w2_piece(I - 6, SLOT);
      if (I < 22) seed_piece(I); else kx_piece(I - 22);
    });
    STAMP(17);
    wait_flags(pfx, tag);
    WAIT_V(0);
    STAMP(29);
    BAR();
    STAMP(30);
    load_u();
    sfor<48, 52>([&](auto I) AI { w2_rd(I); });
    w2_base(-SLOT);
    sfor<52, 56>([&](auto I) AI { w2_rd(I); });
    w2_base(-2 * SLOT);
    BAR();
#pragma unroll
    for (int i = 0; i < 8; ++i) kt_store(i);
    sfor<0, 12>([&](auto I) AI { w2_rd(I); });
    w2_base(SLOT);
    window(IC<0>{});
  }
  STAMP(34);
  BAR();
  STAMP(35);
  sfor<0, 8>([&](auto I) AI { kt_fin(I); if (I >= 4 && I < 7) w2_piece(18 + I - 4, SLOT); });
  const f32x4 csc = lds_ld<f32x4>(LDS_SC);
  STAMP(39);
  WAIT_V(3);
  STAMP(40);
  publish(1);
  BAR();
  STAMP(6);
  rsrc_t rh = mk_rsrc(P.ws + WS_H, H_BYTES);
  const u32 hso = (u32)(((tt * 2 + ch) * 112 + v4 * 28) * 1024);
  u32x4 hf[28];
  auto load_h = [&](int i) AI { hf[i] = ld16_async(rh, lane * 16, hso + g2_k(i) * 1024); };
  const int trow = tt * 64 + ch * 32 + (lane & 31), dcol = nb2 * 64 + 16 * v4 + 8 * lh;
  u32x4 xres;
  f32x4 ud[2];
  u32x2 uu[2];
  {
    f32x4 ya[8];
#pragma unroll
    for (int T = 0; T < 8; ++T) ya[T] = f32x4{0.f, 0.f, 0.f, 0.f};
    auto rdA = [&](int ph, int mu) AI {
      const u32 a = abase + ph * 64 + mu * 512;
      return u32x4{lds_ld<u32>(a), lds_ld<u32>(a + 4), lds_ld<u32>(a + 8), lds_ld<u32>(a + 12)};
    };
    u64 fh = 0;
    u32x4 B[8], A[3];
    auto put_y = [&](int T) AI { lds_st<f32x4>(SCR + ((T ^ 4) * 8 + w) * 1024 + lane * 16, ya[T]); };
    auto conv = [&](auto HALF) AI {
    constexpr int CHK = HALF == 0 ? CHK0 : CHKC, STEP = HALF == 0 ? 3 : 1;
    asm volatile("" ::: "memory");
    A[0] = rdA(0, 0);
#pragma unroll
    for (int g = 7; g >= 0; --g) B[g] = lds_ld<u32x4>(bbase + 64 + g * 512);
    A[1] = rdA(0, 1);
    sfor<0, 16>([&](auto I) AI {
      constexpr int st = I, ph = st >> 3, mu = st & 7;
      SCHED();
      if (st + 2 < 16) A[(st + 2) % 3] = rdA((st + 2) >> 3, (st + 2) & 7);
      sfor<0, 8 - mu>([&](auto J) AI {
        constexpr int T = 7 - J, c = 36 * ph + mu * 8 - mu * (mu - 1) / 2 + J;
        SCHED();
        ya[T] = mma_h(A[st % 3], B[T - mu], ya[T]);
        SCHED();
        if (T == 7 && ph == 0) B[7 - mu] = lds_ld<u32x4>(bbase + (7 - mu) * 512);
        if (c == 1 || c == 5 || c == 9) w2_piece(21 + c / 4, SLOT);
        if (c == 0) asm volatile("buffer_load_dwordx4 %0, %1, %2, 0 offen" : "=v"(xres) : "v"((u32)((trow * D + dcol) * 2)), "s"(rx) : "memory");
        if (c == CHK - 14) asm volatile("global_load_dwordx2 %0, %1, %2 sc1" : "=v"(fh) : "v"(o_fh), "s"(P.ws + WS_FH) : "memory");
        if (c == CHK) {
          STAMP(41);
          WAIT_V(0);
          PIN(fh);
          if (HALF == 1 && b == 255 && w == 7) {
            const char* p = P.ws + WS_FH + (lg * 256 + (lc < 14 ? lc : 0) * 4) * FS;
#pragma unroll 1
            for (u32 sp = 0; sp < SPIN_CAP; ++sp) {
              const u64 v0 = ld_agent(p), v1 = ld_agent(p + 1024 * FS), v2 = ld_agent(p + 2048 * FS), v3 = ld_agent(p + 3072 * FS);
              if (all_of(((v0 ^ tag) | (v1 ^ tag) | (v2 ^ tag) | (v3 ^ tag)) == 0)) break;
              __builtin_amdgcn_s_sleep(1);
            }
            if (lane == 0) st_agent(P.ws + WS_EP, tag);
          }
          if (!all_of(fh == tag)) wait_flags(P.ws + WS_FH + o_fh, tag);
          STAMP(8);
        }
        if (c >= CHK && (c - CHK) % STEP == 0 && (c - CHK) / STEP < HW) load_h((c - CHK) / STEP);
      });
      SCHED();
      if (st == 12) {
#pragma unroll
        for (int i = 0; i < 2; ++i) uu[i] = lds_ld<u32x2>(LDS_U + i * U_CH + 32 + (256 * w + 16 * lc + 4 * lg) * 2);
      }
      if (st == 13) BAR();
      if (st >= 9) put_y(mu - 1);
    });
    STAMP(7);
    put_y(7);
#pragma unroll
    for (int i = (71 - CHK) / STEP + 1; i < HW; ++i) load_h(i);
    };
    if (ch == 0) conv(IC<0>{}); else conv(IC<1>{});
#pragma unroll
    for (int i = 0; i < 2; ++i) {
#pragma unroll
      for (int r = 0; r < 4; ++r) ud[i][r] = h2f((uu[i][r >> 1] >> (16 * (r & 1))) & 0xffff) * csc[2 * i + 1];
      PIN(ud[i]);
    }
    BAR();
  }
  STAMP(9); CSTAMP(61);
  {
    f32x16 oa[2];
#pragma unroll
    for (int i = 0; i < 16; ++i) { oa[0][i] = 0.f; oa[1][i] = 0.f; }
    rsrc_t ra = mk_rsrc(P.ws + WS_AO, AO_BYTES);
    u32x2 ao[4];
    const u32 aoff = (u32)((dcol >> 1) * AO_ROW + (trow >> 8) * 2048 + ((trow >> 1) & 1) * 1024 + ((trow >> 2) & 63) * 16 + (trow & 1) * 8);
    f32x4 y[8];
    w2_base(-SLOT);
    if (G2PRIO && ch) __builtin_amdgcn_s_setprio(1);
    const u32 own = SCR + (w ^ 4) * 8192 + lane * 16;
#pragma unroll
    for (int i = 0; i < 8; ++i) y[i] = lds_ld<f32x4>(own + i * 1024);
#pragma unroll
    for (int i = 0; i < 2; ++i) {
      y[i] = (y[4 * i] + y[4 * i + 1]) + (y[4 * i + 2] + y[4 * i + 3]);
#pragma unroll
      for (int r = 0; r < 4; ++r) y[i][r] = FMA(y[i][r], csc[2 * i], ud[i][r]);
    }
#pragma unroll
    for (int i = 0; i < 2; ++i)
      __builtin_amdgcn_raw_buffer_store_b128(u32x4{pk_bf16(y[0][2 * i], y[1][2 * i]), t32, pk_bf16(y[0][2 * i + 1], y[1][2 * i + 1]), t32}, ra,
                                             b * AO_ROW + w * 2048 + (lc * 4 + lg) * 16 + i * 1024, 0, 16);
    sfor<0, 24>([&](auto I) AI {
      constexpr int s = I;
      if (HW + RATE * s < 28) load_h(HW + RATE * s);
      if (RATE == 2 && HW + 2 * s + 1 < 28) load_h(HW + 2 * s + 1);
      asm volatile("s_waitcnt vmcnt(%1)" : "+v"(hf[s]) : "n"(g2_wait_hf(s)) : "memory");
      if (s == 0) STAMP(46);
      if (s == 5) STAMP(49);
      if (s == 20) STAMP(51);
      if (s + PD == 22) w2_base(SLOT);
#pragma unroll
      for (int i = 0; i < 2; ++i) {
        SCHED();
        oa[i] = mma32(wr[g2_k(s)][i], hf[s], oa[i]);
        SCHED();
        if (s >= 20 && i == 0)
          asm volatile("buffer_load_dwordx2 %0, %1, %2, %3 offen sc1" : "=v"(ao[s - 20]) : "v"(aoff), "s"(ra), "s"((s - 20) * AO_ROW) : "memory");
        if (s + PD >= 16 && s + PD < 28) w2_rd(2 * g2_k(s + PD) + i);
      }
    });
    auto put_o = [&](int j) AI {
      const int g = 1 + (j >> 1), o = (g & 1) * 8 + (j & 1) * 4;
      const f32x16& A = oa[g >> 1];
      lds_st<f32x4>(own + j * 1024, f32x4{A[o], A[o + 1], A[o + 2], A[o + 3]});
    };
    asm volatile("s_waitcnt vmcnt(4)" : "+v"(hf[24]), "+v"(hf[25]), "+v"(hf[26]), "+v"(hf[27]) :: "memory");
    sfor<24, 28>([&](auto I) AI { oa[1] = mma32(wr[g2_k(I)][1], hf[I], oa[1]); });
    sfor<24, 28>([&](auto I) AI { SCHED(); oa[0] = mma32(wr[g2_k(I)][0], hf[I], oa[0]); SCHED(); if (I > 24) put_o(I - 23); });
    put_o(5);
    STAMP(10);
    put_o(0); put_o(1);
    WAIT_V(0);
    PIN(xres);
#pragma unroll
    for (int e = 0; e < 4; ++e) PIN(ao[e]);
    auto ok = [&]() AI { return all_of(((ao[0][1] ^ t32) | (ao[1][1] ^ t32) | (ao[2][1] ^ t32) | (ao[3][1] ^ t32)) == 0); };
    if (__builtin_expect(!ok(), 0)) {
#pragma unroll 1
      for (u32 sp = 0; sp < SPIN_CAP; ++sp) {
#pragma unroll
        for (int e = 0; e < 4; ++e) ao[e] = __builtin_amdgcn_raw_buffer_load_b64(ra, aoff, e * AO_ROW, 16);
        if (ok()) break;
        __builtin_amdgcn_s_sleep(1);
        asm volatile("" ::: "memory");
      }
    }
    STAMP(52);
    float r[8];
#pragma unroll
    for (int e = 0; e < 4; ++e) {
      r[2 * e] = oa[0][2 * e] + (bf2f(xres[e]) + bf2f(ao[e][0])); r[2 * e + 1] = oa[0][2 * e + 1] + (bfhi(xres[e]) + bfhi(ao[e][0]));
      PIN2(r[2 * e], r[2 * e + 1]);
    }
    BAR();
    f32x4 p[6];
#pragma unroll
    for (int g = 1; g < 4; ++g) {
      const u32 a = SCR + (w ^ 4 ^ g) * 8192 + lane * 16 + (g - 1) * 2048;
      p[2 * g - 2] = lds_ld<f32x4>(a); p[2 * g - 1] = lds_ld<f32x4>(a + 1024);
    }
    SCHED();
#pragma unroll
    for (int g = 0; g < 3; ++g)
#pragma unroll
      for (int e = 0; e < 4; ++e) { r[e] += p[2 * g][e]; r[4 + e] += p[2 * g + 1][e]; }
    __builtin_amdgcn_raw_buffer_store_b128(u32x4{pk_bf16(r[0], r[1]), pk_bf16(r[2], r[3]), pk_bf16(r[4], r[5]), pk_bf16(r[6], r[7])}, mk_rsrc(P.out, L * D * 2), (trow * D + dcol) * 2, 0, 0);
    STAMP(11); CSTAMP(62);
    STAMP_OUT;
  }
}

#if defined(__HIP_DEVICE_COMPILE__) && !PKEPI
#pragma clang attribute pop
#endif
extern "C" void kernel_launch(void* const* d_in, const int* in_sizes, int n_in,
                              void* d_out, int out_size, void* d_ws, size_t ws_size,
                              hipStream_t stream) {
  Params P;
  P.x = (const u16*)d_in[0]; P.lar = (const u16*)d_in[1]; P.aim = (const u16*)d_in[2];
  P.cre = (const u16*)d_in[3]; P.cim = (const u16*)d_in[4]; P.ldt = (const u16*)d_in[5];
  P.dsk = (const u16*)d_in[6]; P.w1 = (const u16*)d_in[8]; P.w3 = (const u16*)d_in[9];
  P.w2 = (const u16*)d_in[10]; P.out = (u16*)d_out; P.ws = (char*)d_ws;
  (void)hipFuncSetAttribute((const void*)fused, hipFuncAttributeMaxDynamicSharedMemorySize, LDS_TOTAL);
  fused<<<256, 512, LDS_TOTAL, stream>>>(P);
}
```
